```python
import torch

def _softmax(x, axis=-1):
    return torch.softmax(x, dim=axis)

def _gelu(x):
    return torch.nn.functional.gelu(x, approximate='tanh')

def _silu(x):
    return torch.nn.functional.silu(x)

def _sigmoid(x):
    return torch.sigmoid(x)

def _layernorm(x, axis=-1, eps=1e-05):
    m = x.mean(dim=axis, keepdim=True)
    v = x.var(dim=axis, keepdim=True, unbiased=False)
    return (x - m) / torch.sqrt(v + eps)

def _scatter_add(tgt, idx, src, axis=0):
    return tgt.clone().index_add_(axis, idx, src)

def _segment_sum(src, seg, n):
    out = torch.zeros((n,) + src.shape[1:], dtype=src.dtype)
    return out.index_add_(0, seg, src)

def _pad(x, pw):
    flat = tuple((v for pair in reversed(pw) for v in pair))
    return torch.nn.functional.pad(x, flat)
kernel_name = 'interleave_layers__memory_aug_recurrent_mem_tr__swiglu_ffn__prompt'

def setup_inputs():

    _w = lambda a: (lambda t: t.to(torch.bfloat16) if t.is_floating_point() else t)(torch.as_tensor(a))

    def _A_setup():
        B, S, Dm = (1, 2048, 448)
        H, Dh = (16, 256)
        M, seg = (8, 32)
        seg = min(seg, S)
        S = S // seg * seg
        return {'x': _w((lambda *s: torch.randn(*s, dtype=torch.bfloat16) * 0.05)(B, S, Dm)), 'mem0': _w((lambda *s: torch.randn(*s, dtype=torch.bfloat16) * 0.8)(B, M, Dm)), 'Wq': _w((lambda *s: torch.randn(*s, dtype=torch.bfloat16) * 1.6)(Dm, H, Dh) * Dm ** (-0.5)), 'Wk': _w((lambda *s: torch.randn(*s, dtype=torch.bfloat16) * 1.6)(Dm, H, Dh) * Dm ** (-0.5)), 'Wv': _w((lambda *s: torch.randn(*s, dtype=torch.bfloat16) * 0.8)(Dm, H, Dh) * Dm ** (-0.5)), 'Wo': _w((lambda *s: torch.randn(*s, dtype=torch.bfloat16) * 0.8)(H, Dh, Dm) * (H * Dh) ** (-0.5)), 'seg_len': seg}
    _A_in = _A_setup()

    def _B_setup():
        B, S, D, F = (1, 2048, 448, 1792)
        return {'x': _w((lambda *s: torch.randn(*s, dtype=torch.bfloat16) * 0.05)(B, S, D)), 'w_gate': _w((lambda *s: torch.randn(*s, dtype=torch.bfloat16) * 0.05)(D, F)), 'w_up': _w((lambda *s: torch.randn(*s, dtype=torch.bfloat16) * 0.05)(D, F)), 'w_down': _w((lambda *s: torch.randn(*s, dtype=torch.bfloat16) * 0.05)(F, D))}
    _B_in = _B_setup()
    return {'A_x': _A_in['x'], 'A_mem0': _A_in['mem0'], 'A_Wq': _A_in['Wq'], 'A_Wk': _A_in['Wk'], 'A_Wv': _A_in['Wv'], 'A_Wo': _A_in['Wo'], 'A_seg_len': _A_in['seg_len'], 'B_x': _B_in['x'], 'B_w_gate': _B_in['w_gate'], 'B_w_up': _B_in['w_up'], 'B_w_down': _B_in['w_down']}

def reference(A_x, A_mem0, A_Wq, A_Wk, A_Wv, A_Wo, A_seg_len, B_x, B_w_gate, B_w_up, B_w_down):

    def _A_ref(x, mem0, Wq, Wk, Wv, Wo, seg_len):
        B, S, Dm = x.shape
        H, Dh = (Wq.shape[1], Wq.shape[2])
        M = mem0.shape[1]
        scale = Dh ** (-0.5)
        n_seg = S // seg_len
        L = M + seg_len
        idx = torch.arange(L)
        mask = (idx[:, None] < M) | (idx[:, None] >= idx[None, :])
        mem = mem0
        outs = []
        for s in range(n_seg):
            xs = x[:, s * seg_len:(s + 1) * seg_len, :].clone()
            h = torch.concatenate([mem, xs], axis=1)
            q = torch.einsum('btd,dhe->bhte', h, Wq)
            k = torch.einsum('btd,dhe->bhte', h, Wk)
            v = torch.einsum('btd,dhe->bhte', h, Wv)
            scores = torch.einsum('bhqe,bhke->bhqk', q, k) * scale
            scores = torch.where(mask[None, None], scores, -1000000000.0)
            p = _softmax(scores, axis=-1)
            o = torch.einsum('bhqk,bhke->bhqe', p, v)
            o = torch.einsum('bhte,hed->btd', o, Wo)
            mem = o[:, :M, :].clone()
            outs.append(o[:, M:, :])
        return torch.concatenate(outs, axis=1)

    def _B_ref(x, w_gate, w_up, w_down):
        g = _silu(x @ w_gate)
        u = x @ w_up
        return g * u @ w_down
    x = A_x
    x = x + _A_ref(_layernorm(x, axis=-1), A_mem0, A_Wq, A_Wk, A_Wv, A_Wo, A_seg_len)
    x = x + _B_ref(_layernorm(x, axis=-1), B_w_gate, B_w_up, B_w_down)
    x = x + _A_ref(_layernorm(x, axis=-1), A_mem0, A_Wq, A_Wk, A_Wv, A_Wo, A_seg_len)
    x = x + _B_ref(_layernorm(x, axis=-1), B_w_gate, B_w_up, B_w_down)
    return x
```

```cpp
#define F1W 26000
#include <hip/hip_runtime.h>
#include <hip/hip_bf16.h>

typedef unsigned short u16;
typedef __bf16 bf16x2 __attribute__((ext_vector_type(2)));
typedef __bf16 bf16x4 __attribute__((ext_vector_type(4)));
typedef __bf16 bf16x8 __attribute__((ext_vector_type(8)));
typedef __bf16 bf16x16 __attribute__((ext_vector_type(16)));
typedef short s16x4 __attribute__((ext_vector_type(4)));
typedef float f32x2 __attribute__((ext_vector_type(2)));
typedef float f32x4 __attribute__((ext_vector_type(4)));
typedef unsigned v2u __attribute__((ext_vector_type(2)));
typedef unsigned v4u __attribute__((ext_vector_type(4)));
typedef unsigned v8u __attribute__((ext_vector_type(8)));
#define DI __device__ __forceinline__
#define LAS __attribute__((address_space(3)))
#define SB() __builtin_amdgcn_sched_barrier(0)
typedef __amdgpu_buffer_rsrc_t rsrc_t;

#define MSPLIT 0
#define GENTLE 3
#define WARM 17000
#define WARM1 40000
#define PREF 1
#define BWAIT 1000
#define FWAIT 35000
#define L1WAIT 36000
#define WS_TOP 0
#define XE 12
#define FMIX 1
#define PSPLIT 1
#define FNOP 0
#define NCOPY 3
#define GNOP 2
#define SP0 -1
#define SP1 -1
#define STP(n) do { if (SP0 == n) STAMP(s, 0); if (SP1 == n) STAMP(s, 2); } while (0)
#define PVNOP 1
#define F1X 58
#define F1POLL 1
#define DTP 132
#define DTS 160
#define DTM 4
#define DTX 190
#define DTX2 148
#define DTE 52
#define DTF 93
#define MBN 17
#define BELM 15
#define BNOP 8
#define CNOP 4
#define ENOP 0
#define FS 3
#define FK 6
#define EDELAY 1000
#define MDELAY 0
#define CALFLIP 0
constexpr int SQ = 2048, DM = 448, NH = 16, DH = 256, NSEG = 64, FF = 1792;
constexpr unsigned WA_OFF = 0, WA_SZ = 16 * 4 * 7 * 14 * 1024;
constexpr unsigned WB_OFF = WA_OFF + WA_SZ, WB_SZ = 28 * 4 * 4 * 14 * 1024;
constexpr unsigned XSZ = 28672;
constexpr unsigned CTL_OFF = WB_OFF + WB_SZ;
constexpr unsigned CTL_SZ = 4096;
constexpr unsigned K_E = CTL_OFF, K_CAL = CTL_OFF + 128  , K_FILL = CTL_OFF + 256  ,
                   K_AB = CTL_OFF + 1024  ;
constexpr unsigned CELL = 16384, GRP = 1u << 20;
constexpr unsigned CRIT_OFF = CTL_OFF + CTL_SZ, WS_END = CRIT_OFF + 66 * GRP;
constexpr unsigned C_MEMA = 0, C_MEMB = CELL, C_PM = 2 * CELL, C_PXX = 4 * CELL, C_PXM = 8 * CELL, C_X1 = 9 * CELL, C_XN2 = 13 * CELL,
                   C_XIO = 17 * CELL  , C_XN0 = 21 * CELL, C_PF2 = 25 * CELL, C_BELL = C_PF2 + 4096, C_MEMC = 26 * CELL, C_MEMD = 27 * CELL, C_H = 32 * CELL;
__host__ __device__ constexpr unsigned xmap(unsigned o) { return (o & 8191u) + (o >> 13) * CELL; }
constexpr unsigned SENT = 0xFFFFFFFFu;
constexpr unsigned TB = 64;
constexpr unsigned SPIN_CAP = 1u << 19;

struct Params {
  const u16* x; const u16* mem0; const u16* Wq; const u16* Wk; const u16* Wv; const u16* Wo;
  const u16* wg; const u16* wu; const u16* wd; u16* out; char* ws;
};

DI float b2f_lo(unsigned v) { return __uint_as_float(v << 16); }
DI float b2f_hi(unsigned v) { return __uint_as_float(v & 0xFFFF0000u); }
DI unsigned pk2(float lo, float hi) { f32x2 v = {lo, hi}; return __builtin_bit_cast(unsigned, __builtin_convertvector(v, bf16x2)); }
DI unsigned clean(unsigned v) { return v < 0xFFFFFFFEu ? v : 0xFFFFFFFEu; }
DI float rbf(float f) { return b2f_lo(pk2(f, f)); }
DI void bar() { asm volatile("s_waitcnt lgkmcnt(0)\n\ts_barrier" ::: "memory"); }
template <int CTRL> DI float dpp(float x) { return __builtin_bit_cast(float, __builtin_amdgcn_mov_dpp(__builtin_bit_cast(int, x), CTRL, 0xf, 0xf, true)); }
constexpr int XOR1 = 0xB1, XOR2 = 0x4E, XOR7 = 0x141, XOR8 = 0x128, ROR4 = 0x124, ROR12 = 0x12C;
DI float x16_add(float x) { auto s = __builtin_amdgcn_permlane16_swap(__float_as_uint(x), __float_as_uint(x), false, false); return __uint_as_float(s[0]) + __uint_as_float(s[1]); }
DI float x32_add(float x) { auto s = __builtin_amdgcn_permlane32_swap(__float_as_uint(x), __float_as_uint(x), false, false); return __uint_as_float(s[0]) + __uint_as_float(s[1]); }
DI float x16_max(float x) { auto s = __builtin_amdgcn_permlane16_swap(__float_as_uint(x), __float_as_uint(x), false, false); return fmaxf(__uint_as_float(s[0]), __uint_as_float(s[1])); }
DI float x32_max(float x) { auto s = __builtin_amdgcn_permlane32_swap(__float_as_uint(x), __float_as_uint(x), false, false); return fmaxf(__uint_as_float(s[0]), __uint_as_float(s[1])); }
DI float fold32(float a, float b) {
  auto s = __builtin_amdgcn_permlane32_swap(__float_as_uint(a), __float_as_uint(b), false, false); return __uint_as_float(s[0]) + __uint_as_float(s[1]); }
DI float row16_add(float x) { x += dpp<XOR1>(x); x += dpp<XOR2>(x); x += dpp<XOR7>(x); x += dpp<XOR8>(x); return x; }
DI float row16_max(float x) { x = fmaxf(x, dpp<XOR1>(x)); x = fmaxf(x, dpp<XOR2>(x)); x = fmaxf(x, dpp<XOR7>(x)); x = fmaxf(x, dpp<XOR8>(x)); return x; }
DI bf16x8 lo8(bf16x16 w) { return __builtin_shufflevector(w, w, 0, 1, 2, 3, 4, 5, 6, 7); }
DI bf16x8 hi8(bf16x16 w) { return __builtin_shufflevector(w, w, 8, 9, 10, 11, 12, 13, 14, 15); }
DI bf16x16 cat16(v4u a, v4u b) { v8u r = __builtin_shufflevector(a, b, 0, 1, 2, 3, 4, 5, 6, 7); return __builtin_bit_cast(bf16x16, r); }
DI bf16x8 asb8(v4u a) { return __builtin_bit_cast(bf16x8, a); }
DI f32x4 mfma(bf16x8 a, bf16x8 b, f32x4 c) { return __builtin_amdgcn_mfma_f32_16x16x32_bf16(a, b, c, 0, 0, 0); }
DI f32x4 smfma(bf16x8 a, bf16x16 b, f32x4 c, int idx) { return __builtin_amdgcn_smfmac_f32_16x16x64_bf16(a, b, c, idx, 0, 0); }
DI v4u ld16(rsrc_t r, unsigned vo, unsigned so) { return __builtin_amdgcn_raw_buffer_load_b128(r, vo, so, 16); }
DI v2u ld8(rsrc_t r, unsigned vo, unsigned so) { return __builtin_amdgcn_raw_buffer_load_b64(r, vo, so, 16); }
DI v4u ld16p(rsrc_t r, unsigned vo, unsigned so) { return __builtin_amdgcn_raw_buffer_load_b128(r, vo, so, 0); }
DI void st16(rsrc_t r, unsigned vo, unsigned so, v4u v) {
  __builtin_amdgcn_raw_buffer_store_b128(v, r, vo, so, 16); asm volatile("s_nop 1" :: "v"(v) : "memory"); }
DI void st8(rsrc_t r, unsigned vo, unsigned so, v2u v) { __builtin_amdgcn_raw_buffer_store_b64(v, r, vo, so, 16); }
DI void st4(rsrc_t r, unsigned vo, unsigned so, unsigned v) { __builtin_amdgcn_raw_buffer_store_b32(v, r, vo, so, 16); }
DI unsigned umax3(unsigned a, unsigned b, unsigned c) { unsigned m = a > b ? a : b; return m > c ? m : c; }
DI unsigned mx4(unsigned m, v4u v) { return umax3(umax3(m, v.x, v.y), v.z, v.w); }
DI v4u lds16(const LAS char* p) { return *(const LAS v4u*)p; }
DI void sts16(LAS char* p, v4u v) { *(LAS v4u*)p = v; }

DI bool any_sent(unsigned m) { return __builtin_amdgcn_ballot_w64(m == SENT) != 0; }
DI void give_up(unsigned& left) { asm volatile("s_sub_u32 %0, %0, 1\n\ts_cbranch_scc0 1f\n\ts_endpgm\n1:" : "+s"(left) :: "scc"); }
DI v4u poll1(rsrc_t r, unsigned vo, unsigned so) {
  unsigned left = SPIN_CAP;
  for (;;) {
    const v4u v = ld16(r, vo, so);
    asm volatile("" ::: "memory");
    if (!any_sent(mx4(0, v))) return v;
    give_up(left);
    __builtin_amdgcn_s_sleep(1);
  }
}

#define STAMP(s, k)
#define STDECL
#define WSTAMP
#define STFLUSH(s)
struct Crit { unsigned base, va, sel, tok; };
#define TV(k, v) (unsigned)(v)
DI unsigned crit_base(const char* ws) { const unsigned lo = (unsigned)(size_t)ws; return CRIT_OFF + ((0u - (lo + CRIT_OFF)) & (GRP - 1)); }
DI unsigned crit_off(const Crit& c, int s, unsigned flip = 0) { const unsigned o = (unsigned)s * GRP; return c.base + o + ((((c.va + o) >> 21) ^ c.sel ^ flip) & 1u) * 8192u; }
DI unsigned xin_off(const Crit& c, int layer, int s) { return crit_off(c, s) + (layer ? C_XIO : C_XN0); }

DI void mm_av(f32x4& c, bf16x8 a, bf16x8 b) { asm volatile("v_mfma_f32_16x16x32_bf16 %0, %1, %2, %0" : "+v"(c) : "a"(a), "v"(b)); }
DI void mm_va(f32x4& c, bf16x8 a, bf16x8 b) { asm volatile("v_mfma_f32_16x16x32_bf16 %0, %1, %2, %0" : "+v"(c) : "v"(a), "a"(b)); }
DI void mm_vv(f32x4& c, bf16x8 a, bf16x8 b) { asm volatile("v_mfma_f32_16x16x32_bf16 %0, %1, %2, %0" : "+v"(c) : "v"(a), "v"(b)); }
DI void m0_av(f32x4& c, bf16x8 a, bf16x8 b) { asm volatile("v_mfma_f32_16x16x32_bf16 %0, %1, %2, 0" : "=&v"(c) : "a"(a), "v"(b)); }
DI void m0_va(f32x4& c, bf16x8 a, bf16x8 b) { asm volatile("v_mfma_f32_16x16x32_bf16 %0, %1, %2, 0" : "=&v"(c) : "v"(a), "a"(b)); }
DI void m0_vv(f32x4& c, bf16x8 a, bf16x8 b) { asm volatile("v_mfma_f32_16x16x32_bf16 %0, %1, %2, 0" : "=&v"(c) : "v"(a), "v"(b)); }
DI void sm_a(f32x4& c, bf16x8 a, bf16x16 b, int idx) { asm volatile("v_smfmac_f32_16x16x64_bf16 %0, %1, %2, %3" : "+v"(c) : "v"(a), "a"(b), "v"(idx)); }
DI void sm_v(f32x4& c, bf16x8 a, bf16x16 b, int idx) { asm volatile("v_smfmac_f32_16x16x64_bf16 %0, %1, %2, %3" : "+v"(c) : "v"(a), "v"(b), "v"(idx)); }
DI void tr16(f32x4& c, v2u a, s16x4 id) { asm volatile("v_mfma_f32_16x16x16_bf16 %0, %1, %2, 0" : "=&v"(c) : "v"(a), "v"(id)); }
DI unsigned cvt(float lo, float hi) { unsigned r; asm volatile("v_cvt_pk_bf16_f32 %0, %1, %2" : "=v"(r) : "v"(lo), "v"(hi)); return r; }
template <int N> DI void nop() { asm volatile("s_nop %0" :: "i"(N)); }
DI void zero4(f32x4& c) { f32x2 a, b; asm volatile("v_mov_b64 %0, 0\n\tv_mov_b64 %1, 0" : "=v"(a), "=v"(b)); c = __builtin_shufflevector(a, b, 0, 1, 2, 3); }
DI void pin(f32x4& c) { asm volatile("" : "+v"(c)); }
DI f32x2 fold(const f32x4& c) { f32x2 r; const f32x2 a = {c[0], c[1]}, b = {c[2], c[3]}; asm volatile("v_pk_add_f32 %0, %1, %2" : "=v"(r) : "v"(a), "v"(b)); return r; }
DI unsigned foldc(const f32x4& c) { const f32x2 r = fold(c); return cvt(r[0], r[1]); }
#define PMW 256
constexpr unsigned PM_REC = (PMW == 256) ? 1024 : 640, PM_MM = (PMW == 256) ? 128 : 512, PM_MW = (PMW == 256) ? 256 : 32;
#define DPPOP(name, op, ctl) DI float name(float x) { asm volatile("s_nop 1\n\t" op " %0, %0, %0 " ctl " row_mask:0xf bank_mask:0xf" : "+v"(x)); return x; }
DPPOP(max_x1, "v_max_f32_dpp", "quad_perm:[1,0,3,2]") DPPOP(max_x2, "v_max_f32_dpp", "quad_perm:[2,3,0,1]") DPPOP(max_x7, "v_max_f32_dpp", "row_half_mirror") DPPOP(max_x8, "v_max_f32_dpp", "row_ror:8")
DI float max_x16(float x) { float y; asm volatile("v_mov_b32 %1, %0\n\ts_nop 1\n\tv_permlane16_swap_b32 %0, %1\n\tv_max_f32 %0, %0, %1" : "+v"(x), "=&v"(y)); return x; }
DI float max_x32(float x) { float y; asm volatile("v_mov_b32 %1, %0\n\ts_nop 1\n\tv_permlane32_swap_b32 %0, %1\n\tv_max_f32 %0, %0, %1" : "+v"(x), "=&v"(y)); return x; }
#define VOP2(name, op) DI float name(float a, float b) { float r; asm volatile(op " %0, %1, %2" : "=v"(r) : "v"(a), "v"(b)); return r; }
VOP2(a_add, "v_add_f32") VOP2(a_sub, "v_sub_f32") VOP2(a_max, "v_max_f32") VOP2(a_mul, "v_mul_f32")
DI float a_exp(float a) { float r; asm volatile("v_exp_f32 %0, %1" : "=v"(r) : "v"(a)); return r; }
DI float a_rcp(float a) { float r; asm volatile("v_rcp_f32 %0, %1" : "=v"(r) : "v"(a)); return r; }
DI float a_rnd(float a) { float r; asm volatile("v_cvt_pk_bf16_f32 %0, 0, %1" : "=v"(r) : "v"(a)); return r; }
DI float a_mulc(float a, float c) { float r; asm volatile("v_mul_f32 %0, %2, %1" : "=v"(r) : "v"(a), "s"(c)); return r; }
DI float a_add1(float a) { float r; asm volatile("v_add_f32 %0, 1.0, %1" : "=v"(r) : "v"(a)); return r; }
DI void a_mx4(unsigned& m, v4u v) { asm volatile("v_max3_u32 %0, %0, %1, %2\n\tv_max3_u32 %0, %0, %3, %4" : "+v"(m) : "v"(v.x), "v"(v.y), "v"(v.z), "v"(v.w)); }
DI void a_sw32(float& a, float& b) { asm volatile("v_permlane32_swap_b32 %0, %1" : "+v"(a), "+v"(b)); }
DI void a_sw16(float& a, float& b) { asm volatile("v_permlane16_swap_b32 %0, %1" : "+v"(a), "+v"(b)); }
DI float a_mov(float a) { float r; asm volatile("v_mov_b32 %0, %1" : "=v"(r) : "v"(a)); return r; }
DI float a_max3(float a, float b, float c) { float r; asm volatile("v_max3_f32 %0, %1, %2, %3" : "=v"(r) : "v"(a), "v"(b), "v"(c)); return r; }
DI float a_mask(float a, float neg, unsigned long long m) { float r; asm volatile("v_cndmask_b32 %0, %2, %1, %3" : "=v"(r) : "v"(a), "v"(neg), "s"(m)); return r; }
DI float a_fmah(float a, float c) { float r; asm volatile("v_fma_f32 %0, %1, 0.5, %2" : "=v"(r) : "v"(a), "v"(c)); return r; }
DI unsigned a_clean(unsigned a) { unsigned r; asm volatile("v_min_u32 %0, -2, %1" : "=v"(r) : "v"(a)); return r; }
DI unsigned a_sel(unsigned a, unsigned b, unsigned long long m) { unsigned r; asm volatile("v_cndmask_b32 %0, %1, %2, %3" : "=v"(r) : "v"(a), "v"(b), "s"(m)); return r; }
DI unsigned a_selz(unsigned b, unsigned long long m) { unsigned r; asm volatile("v_cndmask_b32 %0, 0, %1, %2" : "=v"(r) : "v"(b), "s"(m)); return r; }
#define DPPN(name, op, ctl) DI void name(float& x) { asm volatile(op " %0, %0, %0 " ctl " row_mask:0xf bank_mask:0xf" : "+v"(x)); }
DPPN(mx1, "v_max_f32_dpp", "quad_perm:[1,0,3,2]") DPPN(mx2, "v_max_f32_dpp", "quad_perm:[2,3,0,1]") DPPN(mx7, "v_max_f32_dpp", "row_half_mirror") DPPN(mx8, "v_max_f32_dpp", "row_ror:8")
DPPN(ad1, "v_add_f32_dpp", "quad_perm:[1,0,3,2]") DPPN(ad2, "v_add_f32_dpp", "quad_perm:[2,3,0,1]") DPPN(ad7, "v_add_f32_dpp", "row_half_mirror") DPPN(ad8, "v_add_f32_dpp", "row_ror:8")
template <class T> DI T* opaque(T* p) { asm volatile("" : "+v"(p)); return p; }
DI float vmax(float a, float b) { float r; asm("v_max_f32 %0, %1, %2" : "=v"(r) : "v"(a), "v"(b)); return r; }
DI float vmax3(float a, float b, float c) { float r; asm("v_max3_f32 %0, %1, %2, %3" : "=v"(r) : "v"(a), "v"(b), "v"(c)); return r; }

constexpr int NFILL = 12;
DI int fill_task(int layer, int step) {
  const int r = (step < NSEG - 1 ? step : NSEG - 1) >> 2;
  const int a = min(2 * r + 3, r + 16), b = max(r, 2 * r - 2);
  return b + (a - b) * layer;
}
DI void ensure(rsrc_t r, unsigned tok, int& known, int layer, int step, int lane) {
  const int need = fill_task(layer, step);
  if (known > need) return;
  unsigned left = SPIN_CAP;
  for (;;) {
    const unsigned u = __builtin_amdgcn_raw_buffer_load_b32(r, (lane & 15) < NFILL ? (lane & 15) * 4 : 0, K_FILL, 16) - tok;
    asm volatile("" ::: "memory");
    int v = u <= 32u ? (int)u : 0;
    v = min(v, __builtin_amdgcn_mov_dpp(v, XOR1, 0xf, 0xf, true)); v = min(v, __builtin_amdgcn_mov_dpp(v, XOR2, 0xf, 0xf, true));
    v = min(v, __builtin_amdgcn_mov_dpp(v, XOR7, 0xf, 0xf, true)); v = min(v, __builtin_amdgcn_mov_dpp(v, XOR8, 0xf, 0xf, true));
    known = __builtin_amdgcn_readfirstlane(v);
    if (known > need) return;
    give_up(left);
    __builtin_amdgcn_s_sleep(4);
  }
}
DI unsigned now() { unsigned long long t; asm volatile("s_memtime %0\n\ts_waitcnt lgkmcnt(0)" : "=s"(t) :: "memory"); return (unsigned)t; }
DI void throttle(int on) { asm volatile("s_cmp_eq_u32 %0, 0\n\ts_cbranch_scc1 1f\n\ts_waitcnt vmcnt(%1)\n1:" :: "s"(on), "i"(GENTLE) : "scc", "memory"); }
DI void wait_tok(rsrc_t r, unsigned vo, unsigned so, unsigned tok) {
  unsigned left = SPIN_CAP;
  for (;;) {
    const unsigned v = __builtin_amdgcn_raw_buffer_load_b32(r, vo, so, 16);
    asm volatile("" ::: "memory");
    if (__builtin_amdgcn_ballot_w64(v != tok && (int)vo >= 0) == 0) return;
    give_up(left);
    __builtin_amdgcn_s_sleep(2);
  }
}
DI void post_tok(rsrc_t r, unsigned so, unsigned tok, int tid) {
  asm volatile("s_waitcnt vmcnt(0) lgkmcnt(0)\n\ts_barrier" ::: "memory");
  st4(r, tid == 0 ? 0u : 0x80000000u, so, tok);
}
DI unsigned sel_of(int x, int layer) {
  x += __builtin_amdgcn_mov_dpp(x, XOR1, 0xf, 0xf, true); x += __builtin_amdgcn_mov_dpp(x, XOR2, 0xf, 0xf, true); x += __builtin_amdgcn_mov_dpp(x, XOR7, 0xf, 0xf, true);
  return (__builtin_amdgcn_readfirstlane(x) > 0 ? 1u : 0u) ^ (unsigned)layer ^ (unsigned)CALFLIP;
}
DI unsigned get_sel(rsrc_t r, unsigned tok, int layer, int lane) {
  unsigned left = SPIN_CAP;
  for (;;) {
    const v2u v = ld8(r, (lane & 7) * 8, K_CAL);
    asm volatile("" ::: "memory");
    if (__builtin_amdgcn_ballot_w64(v.y != tok) == 0) return sel_of((int)v.x, layer);
    give_up(left);
    __builtin_amdgcn_s_sleep(2);
  }
}
struct Early { v4u ab; v2u cal; };
DI void early_issue(rsrc_t r, int lane, Early& e) {
  e.ab = ld16(r, lane * 16, K_AB); e.cal = ld8(r, (lane & 7) * 8, K_CAL);
  SB();
}
DI void wait_all(rsrc_t r, unsigned tok, int lane) { for (int k = 0; k < 4; ++k) wait_tok(r, lane * 4, K_AB + k * 256, tok); }
DI void early_ln(rsrc_t r, unsigned tok, int lane, const Early& e) {
  if (__builtin_amdgcn_ballot_w64(e.ab.x != tok || e.ab.y != tok || e.ab.z != tok || e.ab.w != tok) != 0) wait_all(r, tok, lane);
  asm volatile("" ::: "memory");
}
DI unsigned early_sel(rsrc_t r, unsigned tok, int layer, int lane, const Early& e, int& known) {
  known = 1 + 3 * layer;
  if (__builtin_amdgcn_ballot_w64(e.cal.y != tok) != 0) return get_sel(r, tok, layer, lane);
  return sel_of((int)e.cal.x, layer);
}
DI unsigned warm_code(int part, int tid) {
  unsigned long long base, next;
  asm volatile("s_getpc_b64 s[20:21]\n\ts_add_u32 s20, s20, _Z6k_main6Params@rel32@lo+4\n\ts_addc_u32 s21, s21, _Z6k_main6Params@rel32@hi+12\n\t"
               "s_getpc_b64 s[22:23]\n\ts_add_u32 s22, s22, _Z5k_endv@rel32@lo+4\n\ts_addc_u32 s23, s23, _Z5k_endv@rel32@hi+12"
               : "={s[20:21]}"(base), "={s[22:23]}"(next) :: "scc");
  const unsigned len = next - base < (1u << 18) ? (unsigned)(next - base) : 0u;
  const rsrc_t rc = __builtin_amdgcn_make_buffer_rsrc((void*)base, 0, len, 0x00020000);
  return __builtin_amdgcn_raw_buffer_load_b32(rc, ((part & 3) * 256 + tid) * 64u, 0, 0);
}
DI void wait_until(unsigned t0, int d) { while ((int)(now() - t0) < d) {} }

DI v4u x_ld(rsrc_t r, unsigned so, int tid, int k) { return ld16(r, tid * 16, so + xmap(k * 4096)); }
DI void x_issue(rsrc_t r, unsigned so, int tid, v4u (&v)[7]) {
#pragma unroll
  for (int k = 0; k < 7; ++k) { v[k] = x_ld(r, so, tid, k); SB(); }
}
DI void x_check(rsrc_t r, unsigned so, int tid, v4u (&v)[7]) {
  unsigned left = SPIN_CAP;
  for (;;) {
    unsigned m = 0;
#pragma unroll
    for (int k = 0; k < 7; ++k) m = mx4(m, v[k]);
    if (!any_sent(m)) return;
    give_up(left);
    poll1(r, tid * 16, so + xmap(6 * 4096));
    x_issue(r, so, tid, v);
    asm volatile("" ::: "memory");
  }
}
DI void x_store(LAS char* dst, int tid, const v4u (&v)[7]) {
#pragma unroll
  for (int k = 0; k < 7; ++k) sts16(dst + tid * 16 + k * 4096, v[k]);
}
DI unsigned pins(unsigned x) { asm volatile("" : "+s"(x)); return x; }
template <class T> DI void pinv(T& x) { asm volatile("" : "+v"(x)); }

constexpr float LOG2E = 1.4426950408889634f;
constexpr float NEG = -3.0e38f;

template <int N> struct IC { static constexpr int v = N; };
template <int B, int E, class F> DI void sfor(F&& f) { if constexpr (B < E) { f(IC<B>{}); sfor<B + 1, E>(f); } }

struct XSC { unsigned ra, rm, ox, om; unsigned long long v0, v1, v2, v3; };
DI void x_softmax(const XSC& c, rsrc_t R, unsigned so) {
#define DPPX " row_mask:0xf bank_mask:0xf\n\t"
  asm volatile(
      "ds_read_b128 v[244:247], %1\n\t"
      "ds_read_b128 v[248:251], %1 offset:1024\n\t"
      "ds_read_b128 v[228:231], %0\n\t"
      "ds_read_b128 v[232:235], %0 offset:1024\n\t"
      "ds_read_b128 v[236:239], %0 offset:2048\n\t"
      "ds_read_b128 v[240:243], %0 offset:3072\n\t"
      "v_mov_b32 v253, 0xff61b1e6\n\t"
      "s_waitcnt lgkmcnt(4)\n\t"
      "v_pk_add_f32 v[244:245], v[244:245], v[248:249]\n\t"
      "v_pk_add_f32 v[246:247], v[246:247], v[250:251]\n\t"
      "s_waitcnt lgkmcnt(2)\n\t"
      "v_pk_add_f32 v[228:229], v[228:229], v[232:233]\n\t"
      "v_pk_add_f32 v[230:231], v[230:231], v[234:235]\n\t"
      "v_permlane32_swap_b32 v244, v246\n\t"
      "v_permlane32_swap_b32 v245, v247\n\t"
      "s_waitcnt lgkmcnt(0)\n\t"
      "v_pk_add_f32 v[244:245], v[244:245], v[246:247]\n\t"
      "v_pk_add_f32 v[236:237], v[236:237], v[240:241]\n\t"
      "v_pk_add_f32 v[238:239], v[238:239], v[242:243]\n\t"
      "v_add_f32_dpp v244, v244, v244 row_ror:8" DPPX
      "v_add_f32_dpp v245, v245, v245 row_ror:8" DPPX
      "v_pk_add_f32 v[228:229], v[228:229], v[236:237]\n\t"
      "v_pk_add_f32 v[230:231], v[230:231], v[238:239]\n\t"
      "v_cndmask_b32 v228, v253, v228, %6\n\t"
      "v_cndmask_b32 v229, v253, v229, %7\n\t"
      "v_cndmask_b32 v230, v253, v230, %8\n\t"
      "v_cndmask_b32 v231, v253, v231, %9\n\t"
      "v_max3_f32 v252, v244, v245, v228\n\t"
      "v_max3_f32 v253, v229, v230, v231\n\t"
      "v_max_f32 v252, v252, v253\n\ts_nop 1\n\t"
      "v_max_f32_dpp v252, v252, v252 row_ror:8" DPPX
      "v_mov_b32 v253, v252\n\ts_nop 1\n\t"
      "v_permlane16_swap_b32 v252, v253\n\t"
      "v_max_f32 v252, v252, v253\n\t"
      "v_mov_b32 v253, v252\n\ts_nop 1\n\t"
      "v_permlane32_swap_b32 v252, v253\n\t"
      "v_max_f32 v252, v252, v253\n\t"
      "v_pk_add_f32 v[228:229], v[228:229], v[252:253] op_sel_hi:[1,0] neg_lo:[0,1] neg_hi:[0,1]\n\t"
      "v_pk_add_f32 v[230:231], v[230:231], v[252:253] op_sel_hi:[1,0] neg_lo:[0,1] neg_hi:[0,1]\n\t"
      "v_pk_add_f32 v[244:245], v[244:245], v[252:253] op_sel_hi:[1,0] neg_lo:[0,1] neg_hi:[0,1]\n\t"
      "v_exp_f32 v228, v228\n\tv_exp_f32 v229, v229\n\tv_exp_f32 v230, v230\n\tv_exp_f32 v231, v231\n\t"
      "v_exp_f32 v244, v244\n\tv_exp_f32 v245, v245\n\t"
      "v_pk_add_f32 v[232:233], v[228:229], v[230:231]\n\t"
      "v_pk_fma_f32 v[232:233], v[244:245], 0.5, v[232:233] op_sel_hi:[1,0,1]\n\t"
      "v_add_f32 v252, v232, v233\n\ts_nop 1\n\t"
      "v_add_f32_dpp v252, v252, v252 row_ror:8" DPPX
      "v_mov_b32 v253, v252\n\ts_nop 1\n\t"
      "v_permlane16_swap_b32 v252, v253\n\t"
      "v_add_f32 v252, v252, v253\n\t"
      "v_mov_b32 v253, v252\n\ts_nop 1\n\t"
      "v_permlane32_swap_b32 v252, v253\n\t"
      "v_add_f32 v252, v252, v253\n\t"
      "v_rcp_f32 v252, v252\n\ts_nop 0\n\t"
      "v_pk_mul_f32 v[228:229], v[228:229], v[252:253] op_sel_hi:[1,0]\n\t"
      "v_pk_mul_f32 v[230:231], v[230:231], v[252:253] op_sel_hi:[1,0]\n\t"
      "v_pk_mul_f32 v[244:245], v[244:245], v[252:253] op_sel_hi:[1,0]\n\t"
      "v_cvt_pk_bf16_f32 v228, v228, v229\n\t"
      "v_cvt_pk_bf16_f32 v229, v230, v231\n\t"
      "v_cvt_pk_bf16_f32 v244, v244, v245\n\t"
      "v_min_u32 v228, -2, v228\n\t"
      "v_min_u32 v229, -2, v229\n\t"
      "buffer_store_dwordx2 v[228:229], %2, %4, %5 offen sc1\n\t"
      "v_min_u32 v244, -2, v244\n\t"
      "buffer_store_dword v244, %3, %4, %5 offen sc1"
      :: "v"(c.ra), "v"(c.rm), "v"(c.ox), "v"(c.om), "s"(R), "s"(so), "s"(c.v0), "s"(c.v1), "s"(c.v2), "s"(c.v3)
      : "memory", "v228", "v229", "v230", "v231", "v232", "v233", "v234", "v235", "v236", "v237", "v238", "v239", "v240", "v241", "v242", "v243",
        "v244", "v245", "v246", "v247", "v248", "v249", "v250", "v251", "v252", "v253");
}

struct XS { f32x4 r0, r1, r2, r3, m0, m1; float a0, a1, a2, a3, b0, b1, b2, b3, x, y, z, t; unsigned p0, p1, p2; };
constexpr int XS_STEPS = 68;
template <int n> DI void xs_step(XS& v, const XSC& c, rsrc_t R, unsigned so, float neg) {
  if constexpr (n == 0) { v.m0 = *(const LAS f32x4*)(size_t)(c.rm); }
  if constexpr (n == 1) { v.m1 = *(const LAS f32x4*)(size_t)(c.rm + 1024); }
  if constexpr (n == 2) { v.r0 = *(const LAS f32x4*)(size_t)(c.ra); }
  if constexpr (n == 3) { v.r1 = *(const LAS f32x4*)(size_t)(c.ra + 1024); }
  if constexpr (n == 4) { v.r2 = *(const LAS f32x4*)(size_t)(c.ra + 2048); }
  if constexpr (n == 5) { v.r3 = *(const LAS f32x4*)(size_t)(c.ra + 3072); }
  if constexpr (n == 8) { v.b0 = a_add(v.m0[0], v.m1[0]); v.b2 = a_add(v.m0[2], v.m1[2]); }
  if constexpr (n == 9) { v.b1 = a_add(v.m0[1], v.m1[1]); v.b3 = a_add(v.m0[3], v.m1[3]); }
  if constexpr (n == 10) { v.a0 = a_add(v.r0[0], v.r1[0]); v.a1 = a_add(v.r0[1], v.r1[1]); }
  if constexpr (n == 11) { v.a2 = a_add(v.r0[2], v.r1[2]); v.a3 = a_add(v.r0[3], v.r1[3]); }
  if constexpr (n == 12) { a_sw32(v.b0, v.b2); }
  if constexpr (n == 13) { a_sw32(v.b1, v.b3); }
  if constexpr (n == 14) { v.x = a_add(v.r2[0], v.r3[0]); v.y = a_add(v.r2[1], v.r3[1]); }
  if constexpr (n == 15) { v.z = a_add(v.r2[2], v.r3[2]); v.t = a_add(v.r2[3], v.r3[3]); }
  if constexpr (n == 16) { v.b0 = a_add(v.b0, v.b2); }
  if constexpr (n == 17) { v.b1 = a_add(v.b1, v.b3); v.a0 = a_add(v.a0, v.x); }
  if constexpr (n == 18) { v.a1 = a_add(v.a1, v.y); v.a2 = a_add(v.a2, v.z); }
  if constexpr (n == 19) { v.a3 = a_add(v.a3, v.t); }
  if constexpr (n == 20) { ad8(v.b0); }
  if constexpr (n == 21) { ad8(v.b1); v.a0 = a_mask(v.a0, neg, c.v0); }
  if constexpr (n == 22) { v.a1 = a_mask(v.a1, neg, c.v1); }
  if constexpr (n == 23) { v.a2 = a_mask(v.a2, neg, c.v2); }
  if constexpr (n == 24) { v.a3 = a_mask(v.a3, neg, c.v3); }
  if constexpr (n == 25) { v.x = a_max3(v.b0, v.b1, v.a0); }
  if constexpr (n == 26) { v.y = a_max3(v.a1, v.a2, v.a3); }
  if constexpr (n == 27) { v.x = a_max(v.x, v.y); }
  if constexpr (n == 28) { mx8(v.x); }
  if constexpr (n == 29) { v.y = a_mov(v.x); }
  if constexpr (n == 30) { a_sw16(v.x, v.y); }
  if constexpr (n == 31) { v.x = a_max(v.x, v.y); }
  if constexpr (n == 32) { v.y = a_mov(v.x); }
  if constexpr (n == 33) { a_sw32(v.x, v.y); }
  if constexpr (n == 34) { v.x = a_max(v.x, v.y); }
  if constexpr (n == 35) { v.a0 = a_sub(v.a0, v.x); }
  if constexpr (n == 36) { v.a1 = a_sub(v.a1, v.x); v.a2 = a_sub(v.a2, v.x); }
  if constexpr (n == 37) { v.a3 = a_sub(v.a3, v.x); v.b0 = a_sub(v.b0, v.x); }
  if constexpr (n == 38) { v.b1 = a_sub(v.b1, v.x); }
  if constexpr (n == 39) { v.a0 = a_exp(v.a0); }
  if constexpr (n == 40) { v.a1 = a_exp(v.a1); }
  if constexpr (n == 41) { v.a2 = a_exp(v.a2); }
  if constexpr (n == 42) { v.a3 = a_exp(v.a3); }
  if constexpr (n == 43) { v.b0 = a_exp(v.b0); }
  if constexpr (n == 44) { v.b1 = a_exp(v.b1); }
  if constexpr (n == 45) { v.x = a_add(v.a0, v.a2); }
  if constexpr (n == 46) { v.y = a_add(v.a1, v.a3); }
  if constexpr (n == 47) { v.x = a_fmah(v.b0, v.x); }
  if constexpr (n == 48) { v.y = a_fmah(v.b1, v.y); }
  if constexpr (n == 49) { v.x = a_add(v.x, v.y); }
  if constexpr (n == 50) { ad8(v.x); }
  if constexpr (n == 51) { v.y = a_mov(v.x); }
  if constexpr (n == 52) { a_sw16(v.x, v.y); }
  if constexpr (n == 53) { v.x = a_add(v.x, v.y); }
  if constexpr (n == 54) { v.y = a_mov(v.x); }
  if constexpr (n == 55) { a_sw32(v.x, v.y); }
  if constexpr (n == 56) { v.x = a_add(v.x, v.y); }
  if constexpr (n == 57) { v.x = a_rcp(v.x); }
  if constexpr (n == 58) { v.a0 = a_mul(v.a0, v.x); }
  if constexpr (n == 59) { v.a1 = a_mul(v.a1, v.x); v.a2 = a_mul(v.a2, v.x); }
  if constexpr (n == 60) { v.a3 = a_mul(v.a3, v.x); v.b0 = a_mul(v.b0, v.x); }
  if constexpr (n == 61) { v.b1 = a_mul(v.b1, v.x); }
  if constexpr (n == 62) { v.p0 = cvt(v.a0, v.a1); }
  if constexpr (n == 63) { v.p1 = cvt(v.a2, v.a3); v.p2 = cvt(v.b0, v.b1); }
  if constexpr (n == 64) { v.p0 = a_clean(v.p0); }
  if constexpr (n == 65) { v.p1 = a_clean(v.p1); v.p2 = a_clean(v.p2); }
  if constexpr (n == 66) { st8(R, c.ox, so, v2u{v.p0, v.p1}); }
  if constexpr (n == 67) { st4(R, c.om, so, v.p2); }
}

DI void fetch_mem(rsrc_t R, unsigned so, unsigned o0, unsigned o1, unsigned lm0, unsigned lm1) {
  unsigned left = SPIN_CAP;
  asm volatile(
      "1:\n\t"
      "buffer_load_dwordx4 v[244:247], %1, %3, %4 offen sc1\n\t"
      "buffer_load_dwordx4 v[248:251], %2, %3, %4 offen sc1\n\t"
      "s_sub_u32 %0, %0, 1\n\t"
      "s_cbranch_scc0 2f\n\t"
      "s_endpgm\n\t"
      "2:\n\t"
      "s_waitcnt vmcnt(1)\n\t"
      "ds_write_b128 %5, v[244:247]\n\t"
      "v_max3_u32 v252, v244, v245, v246\n\t"
      "s_waitcnt vmcnt(0)\n\t"
      "ds_write_b128 %6, v[248:251]\n\t"
      "v_max3_u32 v253, v247, v248, v249\n\t"
      "v_max3_u32 v252, v252, v250, v251\n\t"
      "v_max_u32 v252, v252, v253\n\t"
      "v_cmp_eq_u32 vcc, -1, v252\n\t"
      "s_waitcnt lgkmcnt(0)\n\t"
      "s_cbranch_vccnz 1b\n\t"
      "s_barrier\n\t"
      : "+s"(left)
      : "v"(o0), "v"(o1), "s"(R), "s"(so), "v"(lm0), "v"(lm1)
      : "memory", "vcc", "scc", "v244", "v245", "v246", "v247", "v248", "v249", "v250", "v251", "v252", "v253");
}

#define FM_CHK(a, b, c, d, e, f, g, h, n1, n0) \
      "s_waitcnt vmcnt(" #n1 ")\n\t" \
      "ds_write_b128 %[l0], v[" #a ":" #d "]\n\t" \
      "v_max3_u32 v252, v" #a ", v" #b ", v" #c "\n\t" \
      "s_waitcnt vmcnt(" #n0 ")\n\t" \
      "ds_write_b128 %[l1], v[" #e ":" #h "]\n\t" \
      "v_max3_u32 v253, v" #d ", v" #e ", v" #f "\n\t" \
      "v_max3_u32 v252, v252, v" #g ", v" #h "\n\t" \
      "v_max_u32 v252, v252, v253\n\t" \
      "v_cmp_eq_u32 vcc, -1, v252\n\t"
#define WAIT_BELL(bv, bo, rs, so, left, dt, tmp) \
      "s_branch 7f\n\t" \
      "6:\n\t" \
      "buffer_load_dword " bv ", " bo ", " rs ", " so " offen sc1\n\t" \
      "s_sub_u32 " left ", " left ", 1\n\t" \
      "s_cbranch_scc0 8f\n\t" \
      "s_endpgm\n\t" \
      "8:\n\t" \
      "s_waitcnt vmcnt(0)\n\t" \
      "7:\n\t" \
      "v_cmp_eq_u32 vcc, -1, " bv "\n\t" \
      "s_cbranch_vccnz 6b\n\t" \
      WAIT_TIME(bv, dt, tmp)
#define WAIT_TIME(bv, dt, tmp) \
      "9:\n\t" \
      "s_memrealtime vcc\n\t" \
      "s_waitcnt lgkmcnt(0)\n\t" \
      "s_bitset0_b32 vcc_lo, 31\n\t" \
      "v_sub_u32 " tmp ", vcc_lo, " bv "\n\t" \
      "v_cmp_gt_u32 vcc, " dt ", " tmp "\n\t" \
      "s_cbranch_vccnz 9b\n\t"
DI void fetch_mem3(rsrc_t R, unsigned bel, unsigned bo, unsigned bv, unsigned dt, unsigned sa, unsigned sb, unsigned sc, unsigned o0, unsigned o1, unsigned lm0, unsigned lm1,
                       v8u& hb, v8u& hc, unsigned long long& t4) {
  unsigned left = SPIN_CAP;
  asm volatile(
      WAIT_BELL("%[bv]", "%[bo]", "%[R]", "%[bel]", "%[left]", "%[dt]", "v252")
      "buffer_load_dwordx4 v[244:247], %[o0], %[R], %[sa] offen sc1\n\t"
      "buffer_load_dwordx4 v[248:251], %[o1], %[R], %[sa] offen sc1\n\t"
      ".rept %c[gap]\n\ts_nop 7\n\t.endr\n\t"
      "buffer_load_dwordx4 v[236:239], %[o0], %[R], %[sb] offen sc1\n\t"
      "buffer_load_dwordx4 v[240:243], %[o1], %[R], %[sb] offen sc1\n\t"
      ".rept %c[gap]\n\ts_nop 7\n\t.endr\n\t"
      "buffer_load_dwordx4 v[228:231], %[o0], %[R], %[sc] offen sc1\n\t"
      "buffer_load_dwordx4 v[232:235], %[o1], %[R], %[sc] offen sc1\n\t"
      FM_CHK(244, 245, 246, 247, 248, 249, 250, 251, 5, 4)
      "s_cbranch_vccz 5f\n\t"
      FM_CHK(236, 237, 238, 239, 240, 241, 242, 243, 3, 2)
      "s_cbranch_vccz 5f\n\t"
      FM_CHK(228, 229, 230, 231, 232, 233, 234, 235, 1, 0)
      "s_cbranch_vccz 5f\n\t"
      "3:\n\t"
      "s_waitcnt lgkmcnt(0)\n\t"
      "buffer_load_dwordx4 v[244:247], %[o0], %[R], %[sa] offen sc1\n\t"
      "buffer_load_dwordx4 v[248:251], %[o1], %[R], %[sa] offen sc1\n\t"
      "s_sub_u32 %[left], %[left], 1\n\t"
      "s_cbranch_scc0 4f\n\t"
      "s_endpgm\n\t"
      "4:\n\t"
      FM_CHK(244, 245, 246, 247, 248, 249, 250, 251, 1, 0)
      "s_cbranch_vccnz 3b\n\t"
      "5:\n\t"
      "s_waitcnt lgkmcnt(0)\n\t"
      "s_barrier\n\t"
      "s_memrealtime s[70:71]\n\t"
      : "=&{v[236:243]}"(hb), "=&{v[228:235]}"(hc), [left] "+s"(left), [bv] "+v"(bv), "=&{s[70:71]}"(t4)
      : [o0] "v"(o0), [o1] "v"(o1), [R] "s"(R), [sa] "s"(sa), [sb] "s"(sb), [sc] "s"(sc), [l0] "v"(lm0), [l1] "v"(lm1), [bo] "v"(bo), [bel] "s"(bel), [dt] "s"(dt), [gap] "i"(CNOP)
      : "memory", "vcc", "scc", "v244", "v245", "v246", "v247", "v248", "v249", "v250", "v251", "v252", "v253");
}

typedef unsigned v16u __attribute__((ext_vector_type(16)));
DI void fetch_mem4(rsrc_t R, unsigned bel, unsigned bo, unsigned bv, unsigned dt, unsigned sa, unsigned sb, unsigned sc, unsigned o0, unsigned lm0, v16u& hb, v16u& hc) {
  unsigned left = SPIN_CAP;
  asm volatile(
      WAIT_BELL("%[bv]", "%[bo]", "%[R]", "%[bel]", "%[left]", "%[dt]", "v252")
      "buffer_load_dwordx4 v[236:239], %[o0], %[R], %[sa] offen offset:0 sc1\n\t"
      "buffer_load_dwordx4 v[240:243], %[o0], %[R], %[sa] offen offset:1024 sc1\n\t"
      "buffer_load_dwordx4 v[244:247], %[o0], %[R], %[sa] offen offset:2048 sc1\n\t"
      "buffer_load_dwordx4 v[248:251], %[o0], %[R], %[sa] offen offset:3072 sc1\n\t"
      ".rept %c[gap]\n\ts_nop 7\n\t.endr\n\t"
      "buffer_load_dwordx4 v[220:223], %[o0], %[R], %[sb] offen offset:0 sc1\n\t"
      "buffer_load_dwordx4 v[224:227], %[o0], %[R], %[sb] offen offset:1024 sc1\n\t"
      "buffer_load_dwordx4 v[228:231], %[o0], %[R], %[sb] offen offset:2048 sc1\n\t"
      "buffer_load_dwordx4 v[232:235], %[o0], %[R], %[sb] offen offset:3072 sc1\n\t"
      ".rept %c[gap]\n\ts_nop 7\n\t.endr\n\t"
      "buffer_load_dwordx4 v[204:207], %[o0], %[R], %[sc] offen offset:0 sc1\n\t"
      "buffer_load_dwordx4 v[208:211], %[o0], %[R], %[sc] offen offset:1024 sc1\n\t"
      "buffer_load_dwordx4 v[212:215], %[o0], %[R], %[sc] offen offset:2048 sc1\n\t"
      "buffer_load_dwordx4 v[216:219], %[o0], %[R], %[sc] offen offset:3072 sc1\n\t"
      "s_waitcnt vmcnt(11)\n\tds_write_b128 %[l0], v[236:239] offset:0\n\t"
      "v_max3_u32 v252, v236, v237, v238\n\tv_max_u32 v252, v252, v239\n\t"
      "s_waitcnt vmcnt(10)\n\tds_write_b128 %[l0], v[240:243] offset:1024\n\t"
      "v_max3_u32 v252, v252, v240, v241\n\tv_max3_u32 v252, v252, v242, v243\n\t"
      "s_waitcnt vmcnt(9)\n\tds_write_b128 %[l0], v[244:247] offset:2048\n\t"
      "v_max3_u32 v252, v252, v244, v245\n\tv_max3_u32 v252, v252, v246, v247\n\t"
      "s_waitcnt vmcnt(8)\n\tds_write_b128 %[l0], v[248:251] offset:3072\n\t"
      "v_max3_u32 v252, v252, v248, v249\n\tv_max3_u32 v252, v252, v250, v251\n\t"
      "v_cmp_eq_u32 vcc, -1, v252\n\t"
      "s_cbranch_vccz 5f\n\t"
      "s_waitcnt vmcnt(7)\n\tds_write_b128 %[l0], v[220:223] offset:0\n\t"
      "v_max3_u32 v252, v220, v221, v222\n\tv_max_u32 v252, v252, v223\n\t"
      "s_waitcnt vmcnt(6)\n\tds_write_b128 %[l0], v[224:227] offset:1024\n\t"
      "v_max3_u32 v252, v252, v224, v225\n\tv_max3_u32 v252, v252, v226, v227\n\t"
      "s_waitcnt vmcnt(5)\n\tds_write_b128 %[l0], v[228:231] offset:2048\n\t"
      "v_max3_u32 v252, v252, v228, v229\n\tv_max3_u32 v252, v252, v230, v231\n\t"
      "s_waitcnt vmcnt(4)\n\tds_write_b128 %[l0], v[232:235] offset:3072\n\t"
      "v_max3_u32 v252, v252, v232, v233\n\tv_max3_u32 v252, v252, v234, v235\n\t"
      "v_cmp_eq_u32 vcc, -1, v252\n\t"
      "s_cbranch_vccz 5f\n\t"
      "s_waitcnt vmcnt(3)\n\tds_write_b128 %[l0], v[204:207] offset:0\n\t"
      "v_max3_u32 v252, v204, v205, v206\n\tv_max_u32 v252, v252, v207\n\t"
      "s_waitcnt vmcnt(2)\n\tds_write_b128 %[l0], v[208:211] offset:1024\n\t"
      "v_max3_u32 v252, v252, v208, v209\n\tv_max3_u32 v252, v252, v210, v211\n\t"
      "s_waitcnt vmcnt(1)\n\tds_write_b128 %[l0], v[212:215] offset:2048\n\t"
      "v_max3_u32 v252, v252, v212, v213\n\tv_max3_u32 v252, v252, v214, v215\n\t"
      "s_waitcnt vmcnt(0)\n\tds_write_b128 %[l0], v[216:219] offset:3072\n\t"
      "v_max3_u32 v252, v252, v216, v217\n\tv_max3_u32 v252, v252, v218, v219\n\t"
      "v_cmp_eq_u32 vcc, -1, v252\n\t"
      "s_cbranch_vccz 5f\n\t"
      "3:\n\t"
      "s_waitcnt lgkmcnt(0)\n\t"
      "buffer_load_dwordx4 v[236:239], %[o0], %[R], %[sa] offen offset:0 sc1\n\t"
      "buffer_load_dwordx4 v[240:243], %[o0], %[R], %[sa] offen offset:1024 sc1\n\t"
      "buffer_load_dwordx4 v[244:247], %[o0], %[R], %[sa] offen offset:2048 sc1\n\t"
      "buffer_load_dwordx4 v[248:251], %[o0], %[R], %[sa] offen offset:3072 sc1\n\t"
      "s_sub_u32 %[left], %[left], 1\n\t"
      "s_cbranch_scc0 4f\n\t"
      "s_endpgm\n\t"
      "4:\n\t"
      "s_waitcnt vmcnt(3)\n\tds_write_b128 %[l0], v[236:239] offset:0\n\t"
      "v_max3_u32 v252, v236, v237, v238\n\tv_max_u32 v252, v252, v239\n\t"
      "s_waitcnt vmcnt(2)\n\tds_write_b128 %[l0], v[240:243] offset:1024\n\t"
      "v_max3_u32 v252, v252, v240, v241\n\tv_max3_u32 v252, v252, v242, v243\n\t"
      "s_waitcnt vmcnt(1)\n\tds_write_b128 %[l0], v[244:247] offset:2048\n\t"
      "v_max3_u32 v252, v252, v244, v245\n\tv_max3_u32 v252, v252, v246, v247\n\t"
      "s_waitcnt vmcnt(0)\n\tds_write_b128 %[l0], v[248:251] offset:3072\n\t"
      "v_max3_u32 v252, v252, v248, v249\n\tv_max3_u32 v252, v252, v250, v251\n\t"
      "v_cmp_eq_u32 vcc, -1, v252\n\t"
      "s_cbranch_vccnz 3b\n\t"
      "5:\n\t"
      "s_waitcnt lgkmcnt(0)\n\t"
      "s_barrier\n\t"
      : "=&{v[220:235]}"(hb), "=&{v[204:219]}"(hc), [left] "+s"(left), [bv] "+v"(bv)
      : [o0] "v"(o0), [R] "s"(R), [sa] "s"(sa), [sb] "s"(sb), [sc] "s"(sc), [l0] "v"(lm0), [bo] "v"(bo), [bel] "s"(bel), [dt] "s"(dt), [gap] "i"(CNOP - 1)
      : "memory", "vcc", "scc", "v236", "v237", "v238", "v239", "v240", "v241", "v242", "v243", "v244", "v245", "v246", "v247", "v248", "v249", "v250", "v251", "v252");
}

#define P_AG(u, k) ((u) * 7 + (k) < 32)
struct PU { unsigned tok, xso, pso, bel, cro, cro1, cro2, crc, crd, dt; int need, s, layer; };
DI f32x2 zero2() { f32x2 a; asm volatile("v_mov_b64 %0, 0" : "=v"(a)); return a; }
DI void p_bulk(const bf16x16 (&W)[6][7], const LAS char* sW, const LAS char* xl, const LAS char* xq, const LAS char* xsh, unsigned long long oddw, LAS char* sSw,
               v4u (&kx)[2][4], f32x4 (&z)[11], bf16x16 (&w6)[7], rsrc_t R, int tid, LAS char* xd, unsigned late_wr,
               const XSC& xc, PU& pu, unsigned dtp, unsigned bel_rd, unsigned& bv, int& known, unsigned pro, LAS char* lds) {
  f32x4 acc[2][7], s00, s10, s11;
  XS xs;
  float neg = NEG; pinv(neg);
  int pg;
  v4u xn[7], wt[2];
  f32x2 zh, zg;
  bf16x8 xf[3], wf[3], q0[4], q1[4];
  xf[0] = asb8(lds16(xl)); wf[0] = asb8(lds16(sW)); xf[1] = asb8(lds16(xl + 1024)); wf[1] = asb8(lds16(sW + 1024));
  unsigned c6a, c6b, xm = 0, xo;
  pinv(xm);
  const int s = pu.s;
  SB();
#define PB_CV(tt, u) do { if constexpr ((u) == 6) { c6a = cvt(acc[tt][6][0], acc[tt][6][1]); c6b = cvt(acc[tt][6][2], acc[tt][6][3]); } \
    else if constexpr ((u) & 1) { kx[tt][((u) >> 1) & 3].z = cvt(acc[tt][u][0], acc[tt][u][1]); kx[tt][((u) >> 1) & 3].w = cvt(acc[tt][u][2], acc[tt][u][3]); } \
    else { kx[tt][((u) >> 1) & 3].x = cvt(acc[tt][u][0], acc[tt][u][1]); kx[tt][((u) >> 1) & 3].y = cvt(acc[tt][u][2], acc[tt][u][3]); } } while (0)
#define PB_Z(q) do { if constexpr ((q) & 1) z[(q) >> 1] = __builtin_shufflevector(zh, zero2(), 0, 1, 2, 3); else zh = zero2(); } while (0)
  sfor<0, 196>([&](auto N) __attribute__((always_inline)) {
    constexpr int n = decltype(N)::v;
    constexpr int st = n < 182 ? n / 7 : 26 + (n & 1), u = n < 182 ? n % 7 : ((n - 182) / 2 + 6) % 7, tt = st / 14, ks = st % 14;
    const bf16x8 x = xf[st % 3];
    if constexpr (u < 6) {
      const bf16x8 a = (ks & 1) ? hi8(W[u][ks >> 1]) : lo8(W[u][ks >> 1]);
      if constexpr (ks == 0) { if constexpr (P_AG(u, 0)) m0_av(acc[tt][u], a, x); else m0_vv(acc[tt][u], a, x); }
      else if constexpr (P_AG(u, ks >> 1)) mm_av(acc[tt][u], a, x); else mm_vv(acc[tt][u], a, x);
    } else { if constexpr (ks == 0) m0_vv(acc[tt][6], wf[st % 3], x); else mm_vv(acc[tt][6], wf[st % 3], x); }
    if constexpr (n < 182) {
      if constexpr (u == 0) { xf[(st + 2) % 3] = asb8(lds16(xl + (st + 2) * 1024)); SB(); }
      if constexpr (u == (st < 12 ? 0 : 1)) { wf[(st + 2) % 3] = asb8(lds16(sW + ((st + 2) % 14) * 1024)); SB(); }
      if constexpr (u >= 1 && st * 6 + u - 1 < XS_STEPS) xs_step<st * 6 + u - 1>(xs, xc, R, pu.pso, neg);
      if constexpr (n == 80) STAMP(s, 11);
      if constexpr (n == 80) pg = (int)__builtin_amdgcn_raw_buffer_load_b32(R, pro, K_FILL, 16);
      constexpr int xk = n == 82 ? 0 : n == 87 ? 1 : n == 89 ? 2 : n == 94 ? 3 : n == 96 ? 4 : n == 101 ? 5 : n == 104 ? 6 : -1;
      constexpr int xp = n == 86 ? 1 : n == 88 ? 2 : n == 93 ? 3 : n == 95 ? 4 : n == 97 ? 5 : n == 102 ? 6 : -1;
      if constexpr (xk == 0) xn[0] = ld16(R, tid * 16, pu.xso);
      if constexpr (xp > 0) xo = pins(pu.xso + xmap(xp * 4096));
      if constexpr (xk > 0) xn[xk > 0 ? xk : 0] = ld16(R, tid * 16, xo);
      if constexpr (n == 90) asm volatile("s_barrier" ::: "memory");
      if constexpr (st == 21 && u == 6) bv = __builtin_amdgcn_raw_buffer_load_b32(R, bel_rd, pu.bel, 16);
      if constexpr (u == 2 && st >= 19 && st < 26) sts16(xd + (st - 19) * 4096, xn[st - 19]);
      if constexpr (u == 4 && st >= 19 && st < 26) a_mx4(xm, xn[st - 19]);
      if constexpr (st == 14 && (u == 2 || u == 5)) PB_CV(0, u == 2 ? 0 : 1);
      if constexpr (st == 15 && u >= 2) PB_CV(0, u);
      if constexpr (st == 16 && u == 2) kx[0][3].x = a_selz(c6a, ~oddw);
      if constexpr (st == 16 && u == 3) kx[0][3].y = a_selz(c6b, ~oddw);
      if constexpr (st == 16 && u == 4) kx[0][3].z = a_selz(c6a, oddw);
      if constexpr (st == 16 && u == 5) kx[0][3].w = a_selz(c6b, oddw);
      if constexpr (st == 16 && u == 6) { pu.cro1 = pins(pu.cro + 4096); pu.cro2 = pins(pu.cro + (C_PF2 - C_PM - CELL)); }
      if constexpr (st == 17 && u == 6) { pu.crc = pins(pu.cro + C_MEMC); pu.crd = pins(pu.cro + C_MEMD); }
      if constexpr (st == 18 && u == 6) pu.dt = pins(s > 0 ? dtp : 0u);
      if constexpr (st == 17 && u >= 2 && u < 6) { constexpr int p = u - 2; q0[p] = asb8(lds16(p < 3 ? xq + p * 1024 : xsh)); }
      if constexpr (st == 18 && u >= 2 && u < 6) { constexpr int p = u - 2; q1[p] = asb8(lds16((p < 3 ? xq + p * 1024 : xsh) + 14336)); }
      if constexpr (st == 19 && u >= 3) { if constexpr (u == 3) m0_vv(s00, asb8(kx[0][0]), q0[0]); else mm_vv(s00, asb8(kx[0][u - 3]), q0[u - 3]); }
      if constexpr (st == 20 && u >= 3) { if constexpr (u == 3) m0_vv(s10, asb8(kx[0][0]), q1[0]); else mm_vv(s10, asb8(kx[0][u - 3]), q1[u - 3]); }
      if constexpr (st == 19 && u == 3) asm volatile("v_subrev_u32 %0, %1, %0" : "+v"(pg) : "s"(pu.tok));
      if constexpr (st == 19 && u == 4) asm volatile("v_cmp_gt_u32 vcc, 33, %0\n\ts_nop 1\n\tv_cndmask_b32 %0, 0, %0, vcc" : "+v"(pg) :: "vcc");
      if constexpr (st == 19 && u == 5) asm volatile("v_min_i32_dpp %0, %0, %0 quad_perm:[1,0,3,2] row_mask:0xf bank_mask:0xf" : "+v"(pg));
      if constexpr (st == 19 && u == 6) asm volatile("v_min_i32_dpp %0, %0, %0 quad_perm:[2,3,0,1] row_mask:0xf bank_mask:0xf" : "+v"(pg));
      if constexpr (st == 20 && u == 3) asm volatile("v_min_i32_dpp %0, %0, %0 row_half_mirror row_mask:0xf bank_mask:0xf" : "+v"(pg));
      if constexpr (st == 20 && u == 5) asm volatile("v_min_i32_dpp %0, %0, %0 row_ror:8 row_mask:0xf bank_mask:0xf" : "+v"(pg));
      if constexpr (st == 20 && u == 6) { int k2; asm volatile("v_readfirstlane_b32 %0, %1" : "=s"(k2) : "v"(pg)); known = max(known, k2); }
      if constexpr (st == 21 && u == 5) { pin(s00); *(LAS f32x4*)(sSw) = s00; }
      if constexpr (st == 22 && u == 5) { pin(s10); *(LAS f32x4*)(sSw + 4096) = s10; }
      if constexpr (st >= 22 && (u == 0 || u == 1 || u == 3)) PB_Z(3 * (st - 22) + (u == 3 ? 2 : u));
      if constexpr (st >= 23 && st < 25 && u >= 5) { if constexpr (u == 5) zg = zero2(); else z[6 + st - 23] = __builtin_shufflevector(zg, zero2(), 0, 1, 2, 3); }
      if constexpr (st == 13 && u == 6) pu.need = (int)pins((unsigned)fill_task(pu.layer, s + 2 + 2 * pu.layer));
      if constexpr (st == 25 && u == 5) asm volatile("v_cmp_eq_u32 vcc, -1, %1\n\ts_or_b32 %0, vcc_lo, vcc_hi" : "=s"(xo) : "v"(xm) : "vcc", "scc");
      if constexpr (st == 25 && u == 6) asm volatile("v_mov_b32 %0, %1" : "=v"(xm) : "s"(xo));
    } else {
      constexpr int m = n - 182;
      if constexpr (m < 2) PB_Z(16 + m);
      if constexpr (m == 2) { *(LAS unsigned*)(size_t)late_wr = xm; SB(); PB_Z(18); }
      if constexpr (m == 3) PB_CV(1, 6);
      if constexpr (m == 4) { kx[1][3].x = a_selz(c6a, ~oddw); kx[1][3].y = a_selz(c6b, ~oddw); }
      if constexpr (m == 5) PB_CV(1, 0);
      if constexpr (m == 6) { kx[1][3].z = a_selz(c6a, oddw); kx[1][3].w = a_selz(c6b, oddw); }
      if constexpr (m == 7) PB_CV(1, 1);
      if constexpr (m == 8) { PB_Z(19); m0_vv(s11, asb8(kx[1][3]), q1[3]); PB_Z(20); }
      if constexpr (m == 9) PB_CV(1, 2);
      if constexpr (m == 10) { PB_Z(21); mm_vv(s11, asb8(kx[1][0]), q1[0]); wt[0] = lds16(sW); }
      if constexpr (m == 11) PB_CV(1, 3);
      if constexpr (m == 12) { w6[0] = cat16(wt[0], lds16(sW + 1024)); }
      if constexpr (m == 13) PB_CV(1, 4);
    }
    SB();
  });
  mm_vv(s11, asb8(kx[1][1]), q1[1]);
  wt[1] = lds16(sW + 2048); SB();
  w6[1] = cat16(wt[1], lds16(sW + 3072)); SB();
  nop<1>();
  PB_CV(1, 5);
  nop<0>();
  mm_vv(s11, asb8(kx[1][2]), q1[2]);
  nop<7>();
  pin(s11);
  *(LAS f32x4*)(sSw + 8192) = s11;
}

DI void role_p(rsrc_t R, LAS char* lds, Crit CR, int layer, int rep, int h, int tid) {
  const int lane = tid & 63, w = __builtin_amdgcn_readfirstlane(tid >> 6), i = lane & 15, g = lane >> 4;
  LAS char* sM = lds;
  LAS char* sW = lds + 8192 + w * 14336 + lane * 16;
  LAS char* sX = lds + 8192 + 57344;
  LAS char* sS = sX + 2 * XSZ;
  LAS float* sSm = (LAS float*)(sS + 20480);
  LAS unsigned* sLate = (LAS unsigned*)(sM + 7168);
  bf16x16 W[6][7];
  Early ea;
  {
    const unsigned base = WA_OFF + (unsigned)(h * 4 + w) * 7 * 14 * 1024;
    v4u x0[7], t[14];
    wait_tok(R, lane < 2 ? (h * 8 + 2 * w + lane) * 4 : 0x80000000u, K_AB, CR.tok);
#pragma unroll
    for (int k = 0; k < 14; ++k) { t[k] = ld16(R, lane * 16 + (k & 1) * 1024, base + (6 * 14 + (k & ~1)) * 1024); SB(); }
#pragma unroll
    for (int q = 0; q < 42; ++q) {
      const int u = q / 7, k = q % 7;
      W[u][k] = cat16(ld16(R, lane * 16, base + (u * 14 + 2 * k) * 1024), ld16(R, lane * 16 + 1024, base + (u * 14 + 2 * k) * 1024));
      throttle(layer);
      if (q == 16) {
#pragma unroll
        for (int z = 0; z < 14; ++z) sts16(sW + z * 1024, t[z]);
        SB();
      }
      if (q == 28) early_issue(R, lane, ea);
      if (q == 38) { early_ln(R, CR.tok, lane, ea); x_issue(R, CR.base + rep * GRP + C_XN0, tid, x0); }
    }
    const unsigned wc = warm_code(h, tid);
    x_store(sX, tid, x0);
    asm volatile("" :: "v"(wc));
  }
  const int sidx = (i & 2) ? 0xEEEE : 0x4444;
  const bool odd = w & 1;
  s16x4 ident;
#pragma unroll
  for (int j = 0; j < 4; ++j) ident[j] = (8 * (j >> 1) + 2 * g + (j & 1) == i) ? (short)0x3F80 : (short)0;
  s16x4 idev = ident, idod = ident;
  if (odd) { idev[2] = 0; idev[3] = 0; } else { idod[2] = 0; idod[3] = 0; }
  const int psh = 12 + (w >> 1);
  const int sq = 4 * (w & 1) + g, ss = i >> 3, sd = i & 3;
  const LAS float* ex_rd = opaque(sSm + sq * 56 + 16 + 2 * i);
  const LAS float* em_rd = opaque(sSm + sq * 56 + 8 * (sd & 1) + 4 * (sd >> 1) + 2 * ss);
  const unsigned ex_off = w < 2 ? C_PM + h * 512 + ((i >> 1) & 3) * 128 + sq * 16 + ss * 8 + (i & 1) * 4 : 0x80000000u;
  const unsigned em_off = (w < 2 && !(i & 4)) ? C_PM + CELL + h * 256 + (4 * (sq >> 1) + 2 * ss + (sq & 1)) * 16 + sd * 4 : 0x80000000u;
  const bool dg = (sd >> 1) == (sq >> 2) && ss == ((sq >> 1) & 1);
  const float dg0 = (dg && !(sq & 1)) ? 1.f : 0.f, dg1 = (dg && (sq & 1)) ? 1.f : 0.f;
  const unsigned long long oddl = __builtin_amdgcn_ballot_w64(lane & 1), oddw = __builtin_amdgcn_ballot_w64(odd);
  LAS float* sm_wr = opaque(sSm + (w * 8 + 2 * g) * 56 + i);
  LAS float* sm_wa = opaque(sSm + (w * 8 + 2 * g) * 56 + (i < 8 ? i : 40 + i));
  LAS float* sm_wb = opaque(sSm + (w * 8 + 2 * g) * 56 + (i < 8 ? 48 + i : i));
  const LAS char* m_rd = opaque(sM + lane * 16);
  const unsigned bel_rd = lane < 16 ? lane * 128 : 0x80000000u, bel_wr = (w == 2 && lane < 2) ? C_BELL + lane * 2048 + h * 128 : 0x80000000u;
  int davg = 80 * 8; unsigned dcor = -10 - TB;
  unsigned dtp = TV(0, DTP) + TB;
  const unsigned pro = i < NFILL ? i * 4 : 0;
#define P_BM(n)
  const unsigned o1 = tid < 192 ? tid * 16 + 4096 : tid * 16;
  const LAS char* qa_rd = sM + (6 * w + g) * 256 + i * 16;
  const LAS char* qb_rd = sM + (g < 2 ? 6 * w + 4 + g : 2 * psh + g - 2) * 256 + i * 16;
  LAS char* sSw = opaque(sS + w * 1024 + lane * 16);
  XSC xc;
  { const int qt = w >> 1, qi = (w & 1) * 8 + (lane & 7), tt = (lane >> 3) & 1, lim = 16 * qt + qi - 16 * tt - 4 * g;
    xc.ra = (unsigned)(size_t)(sS + (tt ? 2 : qt) * 4096 + (16 * g + qi) * 16);
    xc.rm = (unsigned)(size_t)(sS + ((3 + qt) * 4 + 2 * tt) * 1024 + (16 * g + qi) * 16);
    xc.ox = C_PXX + (2 * qt + (h >> 3)) * CELL + (h & 7) * 1024 + (16 * g + qi) * 16 + tt * 8;
    xc.om = tt == 0 ? C_PXM + qt * 4096 + h * 256 + qi * 16 + (g & 1) * 8 + (g >> 1) * 4 : 0x80000000u;
    xc.v0 = __builtin_amdgcn_ballot_w64(0 <= lim); xc.v1 = __builtin_amdgcn_ballot_w64(1 <= lim);
    xc.v2 = __builtin_amdgcn_ballot_w64(2 <= lim); xc.v3 = __builtin_amdgcn_ballot_w64(3 <= lim); }
  WSTAMP;
  int known;
  CR.sel = early_sel(R, CR.tok, layer, lane, ea, known);
  ensure(R, CR.tok, known, layer, rep + 2, lane);
  if (layer) {
    const unsigned xso = xin_off(CR, layer, rep);
    v4u x0[7];
    poll1(R, (lane & 31) * 128, xso + xmap(3 * 4096));
    x_issue(R, xso, tid, x0); x_check(R, xso, tid, x0); x_store(sX, tid, x0);
  }
  __builtin_amdgcn_s_waitcnt(0x0F70);
  unsigned pso = 0x40000000u;
  unsigned gb = CR.base + rep * GRP, hf = crit_off(CR, rep) - gb;
  const unsigned hprev = (crit_off(CR, rep + 1) - (gb + GRP)) ^ hf ^ 8192u, xcell = layer ? C_XIO : C_XN0;
  unsigned xcur = (unsigned)(size_t)(sX + lane * 16), xnxt = (unsigned)(size_t)(sX + XSZ + tid * 16), xflip = XSZ;
  for (int s = rep; s < NSEG; s += 2, gb += 2 * GRP, hf ^= 8192u) {
    STDECL;
    PU pu;
    const unsigned cro = gb + hf, adv = s + 2 < NSEG ? 1u : 0u;
    const unsigned xso = pins(gb + adv * (2 * GRP) + (hf ^ (adv << 13)) + xcell);
    pu.tok = CR.tok; pu.xso = xso; pu.pso = pso; pu.bel = pins(s > 0 ? gb - GRP + (hf ^ hprev) + C_BELL : cro + C_MEMA); pu.cro = cro; pu.s = s; pu.layer = layer;
    v4u km[2], kx[2][4];
    bf16x8 q0[4], q1[4];
    f32x4 acc[11], s0, s1, s2, s3;
    bf16x16 w6[7];
    v4u wt;
    float mxx, lx;
    unsigned long long t4, t7;
    v4u lf;
    bar();
    STAMP(s, 1);
    unsigned bv;
    const LAS char* xq = opaque((const LAS char*)(size_t)(xcur + 3 * w * 1024));
    const LAS char* xsh = opaque((const LAS char*)(size_t)(xcur + psh * 1024));
    p_bulk(W, sW, (const LAS char*)(size_t)xcur, xq, xsh, oddw, sSw, kx, acc, w6, R, tid, (LAS char*)(size_t)xnxt, 7168 + 4 * w, xc, pu, dtp, bel_rd, bv, known, pro, lds);
    if (__builtin_expect(known <= pu.need, 0)) ensure(R, CR.tok, known, layer, s + 2 + 2 * layer, lane);
    const unsigned cro1 = pu.cro1, cro2 = pu.cro2;
    {
      f32x4 &sx0 = acc[7], &sx1 = acc[8], &sma = acc[9], &smb = acc[10];
      asm volatile("" : "+v"(w6[0]), "+v"(w6[1]));
      STAMP(s, 3);
      v8u hb, hc;
      fetch_mem3(R, pu.bel, bel_rd, bv, pu.dt, cro + C_MEMA, pu.crc, pu.crd, tid * 16, o1, tid * 16, o1, hb, hc, t4);
      STAMP(s, 4);
      bf16x8 m[7], qa, qb;
      qa = asb8(lds16(qa_rd)); qb = asb8(lds16(qb_rd)); m[0] = asb8(lds16(m_rd)); m[1] = asb8(lds16(m_rd + 1024));
      SB();
#define P_SM(u, k) do { if (P_AG(u, k)) sm_a(acc[u], m[k], W[u][k], sidx); else sm_v(acc[u], m[k], W[u][k], sidx); } while (0)
#define P_RM(k) do { m[k] = asb8(lds16(m_rd + (k) * 1024)); SB(); } while (0)
#define P_RA(k) do { wt = lds16(sW + (2 * (k)) * 1024); SB(); } while (0)
#define P_RB(k) do { w6[k] = cat16(wt, lds16(sW + (2 * (k) + 1) * 1024)); SB(); } while (0)
#define P_S6(k) sm_v(acc[6], m[k], w6[k], sidx)
      float fa, fb, fc, fd, ga, gb2; f32x2 e0, e1, e2, e3; unsigned ep, tb;
      f32x4 T[4]; v2u pk[4];
#define P_A(u) a_add(acc[u][0], acc[u][2])
#define P_B(u) a_add(acc[u][1], acc[u][3])
#define P_Q0(p) do { q0[p] = asb8(lds16((p) < 3 ? xq + (p) * 1024 : xsh)); SB(); } while (0)
#define P_Q1(p) do { q1[p] = asb8(lds16(((p) < 3 ? xq + (p) * 1024 : xsh) + 14336)); SB(); } while (0)
      sm_v(sx0, qa, cat16(kx[0][0], kx[0][1]), sidx); P_RM(2);
      sm_v(sx1, qa, cat16(kx[1][0], kx[1][1]), sidx); P_RA(2);
      sm_v(sx0, qb, cat16(kx[0][2], kx[0][3]), sidx); P_RB(2);
      asm volatile("s_add_u32 %1, s70, %1\n\ts_bitset0_b32 %1, 31\n\tv_mov_b32 %0, %1" : "=v"(tb), "+s"(dcor) : "{s[70:71]}"(t4) : "scc");
      sm_v(sx1, qb, cat16(kx[1][2], kx[1][3]), sidx); st4(R, bel_wr, cro, tb); SB();
      P_S6(0); P_RM(3); fa = a_add(sx0[0], sx0[2]);
      P_SM(2, 0); fb = a_add(sx0[1], sx0[3]);
      P_SM(3, 0); sm_wr[16] = fa; sm_wr[56 + 16] = fb; SB(); fc = a_add(sx1[0], sx1[2]);
      P_SM(0, 0); P_RA(3); fd = a_add(sx1[1], sx1[3]);
      P_SM(4, 0); sm_wr[32] = fc; sm_wr[56 + 32] = fd; SB();
      P_S6(1); P_RB(3);
      P_SM(2, 1);
      P_SM(3, 1); asm volatile("s_waitcnt lgkmcnt(1)\n\ts_barrier" ::: "memory");
      P_SM(0, 1); e0 = *(const LAS f32x2*)(ex_rd); SB(); e1 = *(const LAS f32x2*)(ex_rd + 8 * 56); SB();
      P_SM(4, 1); e2 = *(const LAS f32x2*)(ex_rd + 16 * 56); SB(); e3 = *(const LAS f32x2*)(ex_rd + 24 * 56); SB();
      P_S6(2); P_RM(4);
      P_SM(2, 2); P_RA(4);
      P_SM(3, 2); P_RB(4);
      P_SM(0, 2); P_RM(5);
      P_SM(4, 2); fa = a_add(e0[0], e1[0]);
      P_S6(3); P_RA(5); fb = a_add(e0[1], e1[1]);
      P_SM(2, 3); P_RB(5); fc = a_add(e2[0], e3[0]);
      P_SM(3, 3); P_RM(6); fd = a_add(e2[1], e3[1]);
      P_SM(0, 3); fa = a_add(fa, fc);
      P_SM(4, 3); fb = a_add(fb, fd);
      P_S6(4); P_RA(6); mxx = a_max(fa, fb);
      P_SM(2, 4); P_RB(6); mx1(mxx);
      P_SM(3, 4); mx2(mxx);
      P_SM(0, 4); mx7(mxx);
      P_SM(4, 4); mx8(mxx);
      P_S6(5); fa = a_sub(fa, mxx);
      P_SM(2, 5); fb = a_sub(fb, mxx);
      P_SM(3, 5); fa = a_exp(fa);
      P_SM(0, 5); fb = a_exp(fb);
      P_SM(4, 5); asm volatile("s_waitcnt vmcnt(1)" :: "{v[236:243]}"(hb), "{v[228:235]}"(hc));
      ep = cvt(fa, fb);
      P_S6(6); ep = a_clean(ep);
      P_SM(2, 6); st4(R, ex_off, cro, ep); SB(); lx = a_add(fa, fb);
      P_SM(3, 6); ga = P_A(6);
      P_SM(0, 6); gb2 = P_B(6);
      P_SM(4, 6); fa = P_A(2);
      P_SM(1, 0); fb = P_B(2);
      P_SM(5, 0); pk[2].y = cvt(ga, gb2);
      P_SM(1, 1); pk[2].x = cvt(fa, fb);
      P_SM(5, 1); fa = P_A(3);
      tr16(T[2], pk[2], idev); fb = P_B(3); pk[3].y = cvt(ga, gb2);
      P_SM(1, 2); pk[3].x = cvt(fa, fb);
      P_SM(5, 2); P_Q0(1); fa = P_A(0);
      tr16(T[3], pk[3], idod); fb = P_B(0); fc = P_A(4);
      P_SM(1, 3); P_Q1(1); fd = P_B(4);
      P_SM(5, 3); P_Q0(3); pk[0].x = cvt(fa, fb);
      P_SM(1, 4); P_Q1(3); pk[0].y = cvt(fc, fd);
      P_SM(5, 4); P_Q0(0); km[1].x = cvt(T[2][0], T[2][1]);
      tr16(T[0], pk[0], ident); km[1].y = cvt(T[2][2], T[2][3]); km[1].z = cvt(T[3][0], T[3][1]);
      P_SM(1, 5); P_Q0(2); km[1].w = cvt(T[3][2], T[3][3]);
      P_SM(5, 5); P_Q1(0); ad1(lx);
      P_SM(1, 6); P_Q1(2); km[0].x = cvt(T[0][0], T[0][1]);
      P_SM(5, 6); lf = *(const LAS v4u*)(sLate); SB(); km[0].y = cvt(T[0][2], T[0][3]);
      m0_vv(s0, asb8(km[1]), q0[1]); fa = P_A(1); fb = P_B(1);
      m0_vv(s1, asb8(km[1]), q1[1]);
      asm volatile("v_add_f32 %2, %4, %6\n\tv_add_f32 %3, %5, %7\n\tv_cvt_pk_bf16_f32 %0, %8, %9\n\tv_cvt_pk_bf16_f32 %1, %2, %3"
                   : "=&v"(pk[1].x), "=&v"(pk[1].y), "=&v"(fc), "=&v"(fd) : "v"(acc[5][0]), "v"(acc[5][1]), "v"(acc[5][2]), "v"(acc[5][3]), "v"(fa), "v"(fb));
      ad7(lx);
      tr16(T[1], pk[1], ident);
      m0_vv(s2, asb8(km[1]), q0[3]);
      m0_vv(s3, asb8(km[1]), q1[3]);
      km[0].z = cvt(T[1][0], T[1][1]); km[0].w = cvt(T[1][2], T[1][3]);
      nop<0>();
      sm_v(sma, qa, cat16(km[0], km[1]), sidx);
      sm_v(smb, qb, cat16(km[0], km[1]), sidx);
      mm_vv(s0, asb8(km[0]), q0[0]);
      fa = a_add(sma[0], sma[2]); fb = a_add(sma[1], sma[3]);
      mm_vv(s2, asb8(km[0]), q0[2]);
      fc = a_add(smb[0], smb[2]); fd = a_add(smb[1], smb[3]);
      sm_wa[0] = fa; sm_wa[56] = fb; SB();
      sm_wb[0] = fc; sm_wb[56] = fd; SB();
      mm_vv(s1, asb8(km[0]), q1[0]);
      mm_vv(s3, asb8(km[0]), q1[2]);
    }
    STAMP(s, 5);
    bar();
    STAMP(s, 6);
    {
      const f32x2 p0 = *(const LAS f32x2*)(em_rd); SB();
      const f32x2 p1 = *(const LAS f32x2*)(em_rd + 8 * 56); SB();
      const f32x2 p2 = *(const LAS f32x2*)(em_rd + 16 * 56); SB();
      const f32x2 p3 = *(const LAS f32x2*)(em_rd + 24 * 56); SB();
      const f32x2 a = (p0 + p1) + (p2 + p3);
      float a0 = a[0], a1 = a[1], mx, c, sum;
      asm volatile(
          "s_nop 0\n\t"
          "v_add_f32_dpp %0, %0, %0 quad_perm:[1,0,3,2] row_mask:0xf bank_mask:0xf\n\t"
          "v_add_f32_dpp %1, %1, %1 quad_perm:[1,0,3,2] row_mask:0xf bank_mask:0xf\n\t"
          "v_max3_f32 %2, %0, %1, %5\n\ts_nop 1\n\t"
          "v_max_f32_dpp %2, %2, %2 quad_perm:[2,3,0,1] row_mask:0xf bank_mask:0xf\n\ts_nop 1\n\t"
          "v_max_f32_dpp %2, %2, %2 row_ror:8 row_mask:0xf bank_mask:0xf\n\t"
          "v_sub_f32 %0, %0, %2\n\tv_sub_f32 %1, %1, %2\n\tv_sub_f32 %3, %5, %2\n\t"
          "v_exp_f32 %0, %0\n\tv_exp_f32 %1, %1\n\tv_exp_f32 %3, %3\n\t"
          "v_add_f32 %4, %0, %1\n\t"
          "v_fmac_f32 %4, %3, %6\n\t"
          "v_mul_f32 %2, %3, %7\n\t"
          "v_cndmask_b32 %0, %0, %2, %9\n\t"
          "v_add_f32_dpp %4, %4, %4 quad_perm:[2,3,0,1] row_mask:0xf bank_mask:0xf\n\t"
          "v_mul_f32 %2, %3, %8\n\t"
          "v_cndmask_b32 %1, %1, %2, %9\n\t"
          "v_add_f32_dpp %4, %4, %4 row_ror:8 row_mask:0xf bank_mask:0xf\n\t"
          "v_rcp_f32 %4, %4\n\ts_nop 0\n\t"
          "v_mul_f32 %0, %0, %4\n\tv_mul_f32 %1, %1, %4\n\t"
          "v_cvt_pk_bf16_f32 %2, %0, %1\n\t"
          "v_min_u32 %2, -2, %2"
          : "+v"(a0), "+v"(a1), "=&v"(mx), "=&v"(c), "=&v"(sum)
          : "v"(mxx), "v"(lx), "v"(dg0), "v"(dg1), "s"(oddl));
      st4(R, em_off, cro, __float_as_uint(mx));
      st4(R, em_off, cro1, __float_as_uint(mx));
      st4(R, em_off, cro2, __float_as_uint(mx));
    }
    SB();
    asm volatile("s_memrealtime s[68:69]" : "={s[68:69]}"(t7));
    STAMP(s, 7); STAMP(s, 10);
    {
      pin(s0); pin(s2);
      *(LAS f32x4*)(sSw + 3 * 4096) = g < 2 ? s0 : s2;
      pin(s1); pin(s3);
      *(LAS f32x4*)(sSw + 4 * 4096) = g < 2 ? s1 : s3;
    }
    STAMP(s, 8);
    pso = cro;
    if (__builtin_expect(__builtin_amdgcn_readfirstlane(lf.x | lf.y | lf.z | lf.w) != 0, 0)) {
      v4u xn[7];
      bar();
      x_issue(R, xso, tid, xn);
      x_softmax(xc, R, cro);
      pso = 0x40000000u;
      x_check(R, xso, tid, xn); x_store((LAS char*)(size_t)xnxt, 0, xn);
      __builtin_amdgcn_s_waitcnt(0x0F70);
    }
    { int d;
      asm volatile("s_waitcnt lgkmcnt(0)\n\ts_sub_u32 %0, s68, s70" : "=s"(d) : "{s[68:69]}"(t7), "{s[70:71]}"(t4) : "scc", "memory");
      d = d < 60 ? 60 : (d > 130 ? 130 : d);
      davg += (d * 8 - davg) >> 2;
      dcor = (unsigned)(((davg + 4) >> 3) - 90 - TB); }
    xcur += xflip; xnxt -= xflip; xflip = 0u - xflip;
    pinv(xcur); pinv(xnxt);
    STAMP(s, 9); STFLUSH(s);
  }
  bar();
  x_softmax(xc, R, pso);
}

template <bool BELL>
DI void m_gemm(const bf16x16 (&W)[4][7], const LAS char* xl, LAS char* dst, rsrc_t R, unsigned bo, unsigned bel, unsigned& bv) {
  f32x4 acc[4][2];
  bf16x8 x0[2], x1[2];
  x0[0] = asb8(lds16(xl)); x1[0] = asb8(lds16(xl + 14336));
  SB();
#pragma unroll
  for (int ks = 0; ks < 14; ++ks) {
#pragma unroll
    for (int u = 0; u < 4; ++u) {
      const bf16x8 b = (ks & 1) ? hi8(W[u][ks >> 1]) : lo8(W[u][ks >> 1]);
      if (ks == 0) m0_va(acc[u][0], x0[0], b); else mm_va(acc[u][0], x0[ks & 1], b);
      if (u == 0 && ks < 13) { x0[(ks + 1) & 1] = asb8(lds16(xl + (ks + 1) * 1024)); SB(); }
      if (ks == 0) m0_va(acc[u][1], x1[0], b); else mm_va(acc[u][1], x1[ks & 1], b);
      if (u == 0 && ks < 13) { x1[(ks + 1) & 1] = asb8(lds16(xl + (15 + ks) * 1024)); SB(); }
    }
  }
  if (BELL) { bv = __builtin_amdgcn_raw_buffer_load_b32(R, bo, bel, 16); SB(); }
  nop<2>();
#pragma unroll
  for (int u = 0; u < 4; ++u)
    sts16(dst + u * 1024, v4u{cvt(acc[u][0][0], acc[u][0][1]), cvt(acc[u][0][2], acc[u][0][3]), cvt(acc[u][1][0], acc[u][1][1]), cvt(acc[u][1][2], acc[u][1][3])});
}

DI void lds_wait_ge(unsigned addr, int a) {
  unsigned left = SPIN_CAP; int x;
  asm volatile("1:\n\tds_read_b32 v244, %2\n\ts_sub_u32 %1, %1, 1\n\ts_cbranch_scc0 2f\n\ts_endpgm\n\t2:\n\ts_waitcnt lgkmcnt(0)\n\t"
               "v_readfirstlane_b32 %0, v244\n\ts_cmp_ge_i32 %0, %3\n\ts_cbranch_scc1 3f\n\ts_sleep 1\n\ts_branch 1b\n\t3:"
               : "=&s"(x), "+s"(left) : "v"(addr), "s"(a) : "memory", "scc", "v244");
}
struct MC { LAS char* sX; LAS char* sV; LAS char* sVM; const LAS char* m_rd; LAS char* vm_wr; unsigned o0, lm0, sF, bo, cnt, one; int sidx, lane, w, xb; };
DI v4u m_xld(rsrc_t R, const MC& c, unsigned xso, int n) { return ld16(R, c.lane * 16 + (n & 1) * 1024, xso + xmap((c.xb + (n & ~1)) * 1024)); }
template <int STAGE>
DI bool m_front(rsrc_t R, const bf16x16 (&W)[4][7], const MC& c, unsigned cro, int cur, v4u (&vf)[20], LAS char* lds, int s, int tid, unsigned bel, unsigned bv0, unsigned dtm, unsigned& bv) {
  f32x4 acc[4];
#pragma unroll
  for (int u = 0; u < 4; ++u) zero4(acc[u]);
  const LAS char* v_rd = opaque(c.sV + cur * 16384 + c.lane * 16);
  STAMP(s, 2);
  v16u hb, hc;
  if (STAGE == 1) bar(); else fetch_mem4(R, bel, c.bo, bv0, s > 0 ? dtm : 0u, cro + C_MEMA, cro + C_MEMC, cro + C_MEMD, c.o0, c.lm0, hb, hc);
  STAMP(s, 3);
  bf16x8 m[7];
#pragma unroll
  for (int k = 0; k < 7; ++k) m[k] = asb8(lds16(c.m_rd + k * 1024));
  const int fl = *(const LAS int*)(size_t)(c.sF + 16);
  SB();
#pragma unroll
  for (int k = 0; k < 7; ++k)
#pragma unroll
    for (int u = 0; u < 4; ++u) {
      const int n = 4 * k + u;
      sm_a(acc[u], m[k], W[u][k], c.sidx);
      if (STAGE != 1 && n == 13) asm volatile("s_waitcnt vmcnt(0)" :: "{v[220:235]}"(hb), "{v[204:219]}"(hc));
      if (n == (STAGE ? 27 : MBN)) { bv = __builtin_amdgcn_raw_buffer_load_b32(R, c.bo, cro + C_BELL + 2048, 16); SB(); }
      if (STAGE != 2 && n >= 6 && n < 22) { vf[n - 6] = lds16(v_rd + (n - 6) * 1024); SB(); }
    }
#pragma unroll
  for (int u = 0; u < 4; ++u) *(LAS unsigned*)(c.vm_wr + u * 256) = foldc(acc[u]);
  asm volatile("ds_add_u32 %0, %1 offset:8" :: "v"(c.cnt), "v"(c.one) : "memory");
  return __builtin_amdgcn_readfirstlane(fl) == s + 2;
}
DI unsigned m_early(rsrc_t R, const MC& c, unsigned xso, LAS char* xd, int sn, unsigned bel) {
  v4u xn[28];
#pragma unroll
  for (int k = 0; k < 28; ++k) { xn[k] = ld16(R, c.lane * 16 + (k & 3) * 1024, xso + xmap((k >> 2) * 4096)); SB(); }
  unsigned mm = 0, bv;
#pragma unroll
  for (int k = 0; k < 28; ++k) {
    if (k == 20) { bv = __builtin_amdgcn_raw_buffer_load_b32(R, c.bo, bel, 16); SB(); }
    sts16(xd + k * 1024 + c.lane * 16, xn[k]); mm = mx4(mm, xn[k]); SB();
  }
  *(LAS int*)(size_t)(c.sF + 16) = any_sent(mm) ? -1 : sn;
  return bv;
}
DI void m_wait_vm(const MC& c, int s) { lds_wait_ge(c.sF + 8, 4 * ((s >> 1) + 1)); }
DI bool m_stage(const MC& c, LAS char* dst, const v4u (&xn)[10]) {
  LAS char* d = dst + c.xb * 1024 + c.lane * 16;
  unsigned mm = 0;
#pragma unroll
  for (int k = 0; k < 10; ++k) { sts16(d + k * 1024, xn[k]); mm = mx4(mm, xn[k]); SB(); }
  const bool ok = !any_sent(mm);
  const unsigned one = (ok && c.lane == 0) ? 1u : 0u;
  asm volatile("s_waitcnt lgkmcnt(0)\n\tds_add_u32 %0, %1" :: "v"(c.cnt), "v"(one) : "memory");
  return ok;
}
DI bool m_late(rsrc_t R, const MC& c, unsigned xso, LAS char* xd, bool once) {
  unsigned left = SPIN_CAP;
  for (;;) {
    v4u x2[10];
#pragma unroll
    for (int k = 0; k < 10; ++k) { x2[k] = m_xld(R, c, xso, k); SB(); }
    asm volatile("" ::: "memory");
    const bool ok = m_stage(c, xd, x2);
    __builtin_amdgcn_s_waitcnt(0x0F70);
    if (ok || once) return ok;
    give_up(left);
    poll1(R, (c.lane & 31) * 128, xso + xmap(3 * 4096));
  }
}
DI void m_pv(rsrc_t R, v2u res, unsigned xo, int s, int qt, int lane, unsigned cro, const v4u (&vf)[20], LAS char* lds, int tid, unsigned bo, unsigned bv, unsigned dtx) {
  STAMP(s, 4);
  unsigned left = SPIN_CAP;
  asm volatile(WAIT_BELL("%1", "%2", "%3", "%4", "%0", "%5", "v244") : "+s"(left), "+v"(bv)
               : "v"(bo), "s"(R), "s"(cro + C_BELL + 2048), "s"(dtx) : "memory", "scc", "vcc", "v244");
  STAMP(s, 7);
  f32x4 o0, o1;
  for (;;) {
    v4u pf[20];
#pragma unroll
    for (int k = 0; k < 4; ++k) { pf[16 + k] = ld16(R, lane * 16 + k * 1024, cro + C_PXM + qt * 4096); SB(); }
#pragma unroll
    for (int k = 0; k < 16; ++k) { pf[k] = ld16(R, lane * 16 + (k & 3) * 1024, cro + C_PXX + (2 * qt + (k >> 3)) * CELL + ((k >> 2) & 1) * 4096); SB(); }
    asm volatile("" ::: "memory");
    SB();
    unsigned mm = 0;
#pragma unroll
    for (int z = 0; z < 20; ++z) {
      const int k = z < 4 ? 16 + z : z - 4;
      if (z == 0) m0_vv(o0, asb8(vf[k]), asb8(pf[k])); else if (z == 1) m0_vv(o1, asb8(vf[k]), asb8(pf[k]));
      else if (z & 1) mm_vv(o1, asb8(vf[k]), asb8(pf[k])); else mm_vv(o0, asb8(vf[k]), asb8(pf[k]));
      a_mx4(mm, pf[k]);
      SB();
    }
    if (!any_sent(mm)) break;
    give_up(left);
    poll1(R, (lane & 31) * 128, cro + C_PXM + qt * 4096);
  }
  STAMP(s, 5);
  nop<3>();
  pin(o0); pin(o1);
  const f32x4 o = o0 + o1;
  const unsigned oa = pk2(o[0], o[1]), ob = pk2(o[2], o[3]);
  const v2u y = {clean(pk2(b2f_lo(res.x) + b2f_lo(oa), b2f_hi(res.x) + b2f_hi(oa))), clean(pk2(b2f_lo(res.y) + b2f_lo(ob), b2f_hi(res.y) + b2f_hi(ob)))};
  st8(R, xo, cro + C_X1, y);
  STAMP(s, 10);
}

DI bool m_final(f32x4& a0, const v4u (&pm)[4], const v4u (&bb)[8], int sidx, unsigned psel, unsigned mo, rsrc_t R, unsigned cna, unsigned cnb, unsigned cnc, unsigned cnd) {
  unsigned mm = 0, pv, t; unsigned long long bad;
#pragma unroll
  for (int k = 0; k < 3; ++k) { sm_v(a0, asb8(pm[k]), cat16(bb[2 * k], bb[2 * k + 1]), sidx); a_mx4(mm, pm[k]); }
  sm_v(a0, asb8(pm[3]), cat16(bb[6], bb[7]), sidx);
  asm volatile(
      "v_max3_u32 %3, %3, %4, %5\n\t"
      "v_max3_u32 %3, %3, %6, %7\n\t"
      "v_cmp_eq_u32 vcc, -1, %3\n\t"
      "s_nop 0\n\t"
      "s_mov_b64 %2, vcc\n\t"
      "s_cbranch_vccnz 1f\n\t"
      "s_nop 1\n\t"
      "v_add_f32 %0, %8, %10\n\t"
      "v_add_f32 %1, %9, %11\n\t"
      "v_cvt_pk_bf16_f32 %0, %0, %1\n\t"
      "s_nop 1\n\t"
      "v_mov_b32_dpp %1, %0 quad_perm:[1,0,3,2] row_mask:0xf bank_mask:0xf\n\t"
      "v_perm_b32 %0, %1, %0, %12\n\t"
      "v_min_u32 %0, -2, %0\n\t"
      "buffer_store_dword %0, %13, %14, %15 offen sc1\n\t"
      "buffer_store_dword %0, %13, %14, %17 offen sc1\n\t"
      "buffer_store_dword %0, %13, %14, %18 offen sc1\n\t"
      "1:"
      : "=&v"(pv), "=&v"(t), "=&s"(bad), "+v"(mm)
      : "v"(pm[3].x), "v"(pm[3].y), "v"(pm[3].z), "v"(pm[3].w), "v"(a0[0]), "v"(a0[1]), "v"(a0[2]), "v"(a0[3]), "v"(psel), "v"(mo), "s"(R), "s"(cna), "s"(cnb), "s"(cnc), "s"(cnd)
      : "memory", "vcc");
  return bad == 0;
}

DI void role_m(rsrc_t R, const Params& P, LAS char* lds, Crit CR, int layer, int rep, int j, int tid) {
  const int lane = tid & 63, w = __builtin_amdgcn_readfirstlane(tid >> 6), i = lane & 15, g = lane >> 4;
  MC c;
  c.sX = lds;
  c.sV = lds + 2 * XSZ;
  c.sVM = c.sV + 32768;
  LAS char* sM = c.sVM + 4096;
  LAS int* sF = (LAS int*)(sM + 8192);
  bf16x16 W[4][7];
  v4u x0[7];
  Early ea;
  wait_tok(R, lane < 8 ? (128 + (4 * w + (lane >> 1)) * 8 + 2 * (j / 7) + (lane & 1)) * 4 : 0x80000000u, K_AB, CR.tok);
  {
    const unsigned base = WB_OFF + (unsigned)(j * 4 + w) * 4 * 14 * 1024;
#pragma unroll
    for (int u = 0; u < 4; ++u)
#pragma unroll
      for (int k = 0; k < 7; ++k) { W[u][k] = cat16(ld16(R, lane * 16, base + (u * 14 + 2 * k) * 1024), ld16(R, lane * 16 + 1024, base + (u * 14 + 2 * k) * 1024)); throttle(layer);
        if (u * 7 + k == 15) early_issue(R, lane, ea);
        if (u * 7 + k == 25) { early_ln(R, CR.tok, lane, ea); x_issue(R, CR.base + rep * GRP + C_XN0, tid, x0); } }
    const unsigned wc = warm_code(j, tid);
    asm volatile("" :: "v"(wc));
  }
  c.sidx = (i & 2) ? 0xEEEE : 0x4444; c.lane = lane; c.w = w; c.xb = w == 1 ? 0 : (w == 2 ? 10 : 18);
  c.o0 = lane * 16 + w * 1024;
  c.lm0 = (unsigned)(size_t)(sM + c.o0); c.sF = (unsigned)(size_t)sF;
  c.bo = lane < 16 ? lane * 128 : 0x80000000u; c.cnt = c.sF + (lane == 0 ? 0u : 32u + 4u * lane); c.one = lane == 0 ? 1u : 0u;
  const unsigned dtm = TV(0, DTP) + TV(1, DTM) + TB;
#define M_BEL (crit_off(CR, s > 0 ? s - 1 : 0) + (s > 0 ? C_BELL : C_MEMA))
  c.m_rd = opaque(sM + lane * 16); c.vm_wr = opaque(c.sVM + 4 * w * 256 + i * 16 + g * 4);
  WSTAMP;
  int known;
  CR.sel = early_sel(R, CR.tok, layer, lane, ea, known);
  ensure(R, CR.tok, known, layer, rep + 2, lane);
  {
    const unsigned xso = xin_off(CR, layer, rep);
    if (layer) { poll1(R, (lane & 31) * 128, xso + xmap(3 * 4096)); x_issue(R, xso, tid, x0); }
    x_check(R, xso, tid, x0); x_store(c.sX, tid, x0);
    if (tid == 0) { sF[0] = 0; sF[1] = -1; sF[2] = 0; sF[4] = -1; }
  }
  bar();
  int late3 = 0;
  if (w == 0) {
    unsigned vox = (i >> 3) * 512 + g * 128 + (i & 7) * 16, l16 = lane * 16;
    unsigned bo = c.bo, dte = TV(2, DTE) + TB, dtf = TV(3, DTF) + TB;
    pinv(vox); pinv(l16); pinv(bo);
    const unsigned mo = j * 256 + (4 * g + (i & 3)) * 16 + (i >> 2) * 4;
    const LAS char* vm_rd = opaque(c.sVM + (g >> 1) * 256 + i * 16 + (g & 1) * 8);
    const unsigned long long hig = __builtin_amdgcn_ballot_w64(g >= 2);
    const unsigned psel = (lane & 1) ? 0x03020706u : 0x05040100u;
    for (int s = rep; s < NSEG; s += 2) {
      STDECL;
      const int cur = (s >> 1) & 1;
      lds_wait_ge(c.sF, late3);
      STAMP(s, 1);
      unsigned bvn;
      m_gemm<true>(W, c.sX + cur * XSZ + lane * 16, c.sV + cur * 16384 + lane * 16, R, c.bo, pins(M_BEL), bvn);
      STAMP(s, 9);
      const unsigned cro = crit_off(CR, s), so = pins(cro + C_PM), so2 = pins(cro + C_PM + 4096), sof = pins(cro + C_PM + CELL), sbel = pins(cro + C_BELL + 2048);
      const unsigned cna = pins(crit_off(CR, s + 1) + C_MEMA), cnb = pins(cna + (C_MEMB - C_MEMA)), cnc = pins(cna + (C_MEMC - C_MEMA)), cnd = pins(cna + (C_MEMD - C_MEMA));
      v4u vf[20];
      unsigned bv;
      const bool early = m_front<0>(R, W, c, cro, cur, vf, lds, s, tid, M_BEL, bvn, dtm, bv);
      if (s + 1 < NSEG) {
        STAMP(s, 0);
        v4u bb[8], pm[NCOPY][4];
        unsigned left = SPIN_CAP;
        f32x4 a0[NCOPY];
        const unsigned sc[3] = {sof, pins(sof + 4096), pins(cro + C_PF2)};
        asm volatile(WAIT_BELL("%1", "%2", "%3", "%4", "%0", "%5", "v244") : "+s"(left), "+v"(bv) : "v"(bo), "s"(R), "s"(sbel), "s"(dte) : "memory", "scc", "vcc", "v244");
        STAMP(s, 4);
        for (;;) {
          v4u ex[8];
#pragma unroll
          for (int k = 0; k < 8; ++k) { ex[k] = ld16(R, vox + (k & 3) * 1024, k < 4 ? so : so2); SB(); }
          asm volatile("" ::: "memory");
          SB();
          m_wait_vm(c, s);
#pragma unroll
          for (int p = 0; p < 8; ++p) { const v2u t = *(const LAS v2u*)(vm_rd + p * 512); bb[p].x = t.x; bb[p].y = t.y; SB(); }
#pragma unroll
          for (int q = 0; q < NCOPY; ++q) zero4(a0[q]);
          SB();
          unsigned mm = 0;
          f32x4 na[8], nb[8];
#pragma unroll
          for (int k = 0; k < 10; ++k) {
            if (k < 8) { m0_vv(na[k], asb8(ex[k]), asb8(vf[2 * k])); a_mx4(mm, ex[k]); m0_vv(nb[k], asb8(ex[k]), asb8(vf[2 * k + 1])); }
            if (k == 8) nop<1>();
#pragma unroll
            for (int q = 0; q < NCOPY; ++q)
              if (k == FK + q * FS) {
                if (q == 0) { asm volatile(WAIT_TIME("%0", "%1", "v244") :: "v"(bv), "s"(dtf) : "memory", "vcc", "v244"); STAMP(s, 7); }
#pragma unroll
                for (int z = 0; z < 4; ++z) pm[q][z] = ld16(R, l16 + z * 1024, sc[q]);
                SB();
              }
            if (k >= 2) {
              const unsigned c0 = cvt(na[k - 2][0], na[k - 2][1]), c1 = cvt(na[k - 2][2], na[k - 2][3]), d0 = cvt(nb[k - 2][0], nb[k - 2][1]), d1 = cvt(nb[k - 2][2], nb[k - 2][3]);
              bb[k - 2].z = a_sel(c0, d0, hig); bb[k - 2].w = a_sel(c1, d1, hig);
            }
            SB();
          }
          if (!any_sent(mm)) break;
          give_up(left);
        }
        pinv(bb[0]); pinv(bb[1]); pinv(bb[2]); pinv(bb[3]); pinv(bb[4]); pinv(bb[5]); pinv(bb[6]); pinv(bb[7]);
        STAMP(s, 8);
#pragma unroll
        for (int q = 0; q < NCOPY; ++q)
          if (FK + q * FS >= 10) {
#pragma unroll
            for (int z = 0; z < 4; ++z) pm[q][z] = ld16(R, l16 + z * 1024, sc[q]);
            SB();
          }
        bool done = false;
#pragma unroll
        for (int q = 0; q < NCOPY; ++q) if (!done) done = m_final(a0[q], pm[q], bb, c.sidx, psel, mo, R, cna, cnb, cnc, cnd);
        while (!done) {
#pragma unroll
          for (int z = 0; z < 4; ++z) pm[0][z] = ld16(R, l16 + z * 1024, sof);
          asm volatile("" ::: "memory");
          give_up(left);
          zero4(a0[0]);
          nop<1>();
          done = m_final(a0[0], pm[0], bb, c.sidx, psel, mo, R, cna, cnb, cnc, cnd);
        }
        STAMP(s, 6); STAMP(s, 10);
      }
      sF[1] = s;
      if (!early) late3 += 3;
      STAMP(s, 11); STFLUSH(s);
    }
  } else {
    unsigned z;
    m_gemm<false>(W, c.sX + lane * 16, c.sV + 4 * w * 1024 + lane * 16, R, 0, 0, z);
    if (w == 3) {
      unsigned bvp = 0;
      for (int s = rep; s < NSEG; s += 2) {
        STDECL;
        const int cur = (s >> 1) & 1;
        const bool more = s + 2 < NSEG;
        const unsigned cro = crit_off(CR, s), xso = xin_off(CR, layer, more ? s + 2 : s), bel = pins(M_BEL);
        LAS char* xd = c.sX + (cur ^ 1) * XSZ;
        ensure(R, CR.tok, known, layer, s + 2, lane);
        asm volatile(WAIT_TIME("%0", "%1", "v244") :: "v"(bvp), "s"(s >= 2 ? TV(6, DTS) + TB : 0u) : "memory", "vcc", "v244");
        STAMP(s, 4);
        const unsigned bv0 = m_early(R, c, xso, xd, s + 2, bel);
        v4u vf[20];
        const bool early = m_front<2>(R, W, c, cro, cur, vf, lds, s, tid, bel, bv0, dtm, bvp);
        STAMP(s, 0);
        if (more) {
          if (__builtin_expect(!early, 0)) { late3 += 3; lds_wait_ge(c.sF + 4, s); m_late(R, c, xso, xd, false); lds_wait_ge(c.sF, late3); }
          STAMP(s, 1);
          m_gemm<false>(W, xd + lane * 16, c.sV + (cur ^ 1) * 16384 + 12 * 1024 + lane * 16, R, 0, 0, z);
          STAMP(s, 9);
        }
        m_wait_vm(c, s);
        STAMP(s, 11); STFLUSH(s);
      }
    } else {
      const rsrc_t RX = __builtin_amdgcn_make_buffer_rsrc(layer ? (void*)P.ws : (void*)P.x, 0, layer ? WS_END : SQ * DM * 2, 0x00020000);
      const int row = 16 * (w - 1) + i;
      const unsigned xo = xmap(j * 1024) + row * 32 + g * 8, ro = layer ? xo : (unsigned)(row * DM + 16 * j + 4 * g) * 2;
      for (int s = rep; s < NSEG; s += 2) {
        STDECL;
        const int cur = (s >> 1) & 1;
        const bool more = s + 2 < NSEG;
        const unsigned cro = crit_off(CR, s);
        LAS char* xd = c.sX + (cur ^ 1) * XSZ;
        LAS char* vd = c.sV + (cur ^ 1) * 16384 + 4 * w * 1024 + lane * 16;
        v4u vf[20];
        unsigned bv;
        const bool early = m_front<1>(R, W, c, cro, cur, vf, lds, s, tid, 0, 0, 0, bv) && more;
        STAMP(s, 0);
        if (early) {
          STAMP(s, 1);
          m_gemm<false>(W, xd + lane * 16, vd, R, 0, 0, z);
          STAMP(s, 9);
        }
        const unsigned xso = xin_off(CR, layer, more ? s + 2 : s), rso = layer ? crit_off(CR, s, 1) + C_XIO : (unsigned)s * XSZ;
        lds_wait_ge(c.sF + 4, s);
        if (s + 1 >= NSEG) m_wait_vm(c, s);
        const v2u res = __builtin_amdgcn_raw_buffer_load_b64(RX, ro, rso, 16);
#pragma unroll
        for (int k = 0; k < 4; ++k) vf[16 + k] = lds16(c.sVM + k * 1024 + lane * 16);
        bool mine = true;
        if (__builtin_expect(!early && more, 0)) mine = m_late(R, c, xso, xd, true);
        m_pv(R, res, xo, s, w - 1, lane, cro, vf, lds, tid, c.bo, bv, (early ? TV(4, DTX) : TV(5, DTX2)) + TB);
        if (__builtin_expect(!early && more, 0)) {
          late3 += 3;
          if (!mine) m_late(R, c, xso, xd, false);
          lds_wait_ge(c.sF, late3);
          m_gemm<false>(W, xd + lane * 16, vd, R, 0, 0, z);
        }
        STAMP(s, 11); STFLUSH(s);
      }
    }
  }
}

DI unsigned ln_word(unsigned v, float mb, float rs) {
  const unsigned d = pk2(b2f_lo(v) - mb, b2f_hi(v) - mb);
  return pk2(b2f_lo(d) * rs, b2f_hi(d) * rs);
}
DI float ln_rs(float var) { return __builtin_amdgcn_rcpf(rbf(__builtin_amdgcn_sqrtf(rbf(rbf(var) + 1e-5f)))); }

DI void role_n(rsrc_t R, LAS char* lds, Crit CR, int layer, unsigned src, unsigned sflip, unsigned dst, int a, int tid) {
  const int lane = tid & 63, w = tid >> 6, m = lane & 31, r8 = 2 * w + (lane >> 5), mc = m < 28 ? m : 27;
  const float keep = m < 28 ? 1.0f / DM : 0.f;
  const unsigned vi = xmap(mc * 1024) + (8 * a + r8) * 32;
  const unsigned ob = (a >> 1) * 14336 + (a & 1) * 128 + (tid >> 3) * 256 + (tid & 7) * 16;
  const unsigned vo0 = xmap(ob), vo1 = tid < 192 ? xmap(ob + 8192) : 0x80000000u;
  CR.sel = get_sel(R, CR.tok, layer, lane);
  WSTAMP;
  int known = 0;
  for (int s = 0; s < NSEG; ++s) {
    v4u v[2];
    STDECL;
    ensure(R, CR.tok, known, layer, s, lane);
    STAMP(s, 0);
    const unsigned si = crit_off(CR, s, sflip) + src, so = crit_off(CR, s) + dst;
    unsigned left = SPIN_CAP;
    for (;;) {
      v[0] = ld16(R, vi, si); v[1] = ld16(R, vi + 16, si);
      asm volatile("" ::: "memory");
      if (!any_sent(mx4(mx4(0, v[0]), v[1]))) break;
      give_up(left);
    }
    STAMP(s, 1);
    f32x2 f[8];
#pragma unroll
    for (int e = 0; e < 8; ++e) f[e] = f32x2{b2f_lo(v[e >> 2][e & 3]), b2f_hi(v[e >> 2][e & 3])};
    const f32x2 s2 = ((f[0] + f[1]) + (f[2] + f[3])) + ((f[4] + f[5]) + (f[6] + f[7]));
    const float mean = x16_add(row16_add((s2[0] + s2[1]) * keep));
    const float mb = rbf(mean);
    f32x2 q2 = {0, 0}, q3 = {0, 0};
    unsigned dp[8];
#pragma unroll
    for (int e = 0; e < 8; e += 2) { const f32x2 d0 = f[e] - mean, d1 = f[e + 1] - mean; q2 += d0 * d0; q3 += d1 * d1; }
    q2 += q3;
    const float var = x16_add(row16_add((q2[0] + q2[1]) * keep));
#pragma unroll
    for (int e = 0; e < 8; ++e) { const f32x2 d = f[e] - mb; dp[e] = pk2(d[0], d[1]); }
    const float rs = ln_rs(var);
    LAS char* buf = lds + (s & 1) * 8192;
#pragma unroll
    for (int k = 0; k < 2; ++k) {
      v4u o;
#pragma unroll
      for (int e = 0; e < 4; ++e) o[e] = clean(pk2(b2f_lo(dp[4 * k + e]) * rs, b2f_hi(dp[4 * k + e]) * rs));
      sts16(buf + (2 * m + k) * 128 + ((r8 ^ m) & 7) * 16, o);
    }
    bar();
    const int xr = ((tid & 7) ^ (tid >> 4)) & 7;
    const v4u o0 = lds16(buf + (tid >> 3) * 128 + xr * 16), o1 = lds16(buf + 4096 + (tid >> 3) * 128 + xr * 16);
    st16(R, vo0, so, o0);
    st16(R, vo1, so, o1);
    STAMP(s, 2); STAMP(s, 11); STFLUSH(s);
  }
}

struct GS { float g0, g1, u0, u1, e0, e1; };
constexpr int GS_STEPS = 12;
template <int n> DI void glu_step(GS& x, const f32x4& g, const f32x4& u, int h, unsigned& out) {
  if constexpr (n == 0) { x.g0 = a_rnd(g[2 * h]); x.g1 = a_rnd(g[2 * h + 1]); }
  if constexpr (n == 1) { x.u0 = a_rnd(u[2 * h]); x.u1 = a_rnd(u[2 * h + 1]); }
  if constexpr (n == 2) { x.e0 = a_mulc(x.g0, -LOG2E); x.e1 = a_mulc(x.g1, -LOG2E); }
  if constexpr (n == 3) x.e0 = a_exp(x.e0);
  if constexpr (n == 4) x.e1 = a_exp(x.e1);
  if constexpr (n == 5) { x.e0 = a_add1(x.e0); x.e1 = a_add1(x.e1); }
  if constexpr (n == 6) x.e0 = a_rcp(x.e0);
  if constexpr (n == 7) x.e1 = a_rcp(x.e1);
  if constexpr (n == 8) { x.g0 = a_mul(x.g0, x.e0); x.g1 = a_mul(x.g1, x.e1); }
  if constexpr (n == 9) { x.g0 = a_rnd(x.g0); x.g1 = a_rnd(x.g1); }
  if constexpr (n == 10) { x.g0 = a_mul(x.g0, x.u0); x.g1 = a_mul(x.g1, x.u1); }
  if constexpr (n == 11) asm volatile("v_cvt_pk_bf16_f32 %0, %1, %2\n\tv_min_u32 %0, -2, %0" : "=v"(out) : "v"(x.g0), "v"(x.g1));
}
DI bf16x8 tr_frag(const LAS char* p, int stride4) {
  const s16x4 lo = __builtin_amdgcn_ds_read_tr16_b64_v4i16((LAS s16x4*)p), hi = __builtin_amdgcn_ds_read_tr16_b64_v4i16((LAS s16x4*)(p + stride4));
  return __builtin_bit_cast(bf16x8, __builtin_shufflevector(lo, hi, 0, 1, 2, 3, 4, 5, 6, 7));
}
DI void role_f1(rsrc_t R, const Params& P, LAS char* lds, Crit CR, int layer, int f, int tid) {
  const int lane = tid & 63, w = __builtin_amdgcn_readfirstlane(tid >> 6), i = lane & 15, g = lane >> 4;
  bf16x8 W[4][14];
#pragma unroll
  for (int mat = 0; mat < 2; ++mat) {
    const u16* src = (mat ? P.wu : P.wg) + (size_t)(tid >> 4) * FF + 128 * f + 8 * (tid & 15);
    v4u t[28];
#pragma unroll
    for (int k = 0; k < 28; ++k) t[k] = *(const v4u*)(src + (size_t)k * 16 * FF);
    if (mat) bar();
#pragma unroll
    for (int k = 0; k < 28; ++k) sts16(lds + ((tid >> 4) + 16 * k) * 272 + (tid & 15) * 16, t[k]);
    bar();
#pragma unroll
    for (int u = 0; u < 2; ++u)
#pragma unroll
      for (int k = 0; k < 14; ++k) { W[2 * mat + u][k] = tr_frag(lds + (32 * k + 8 * g + (i >> 2)) * 272 + (32 * w + 8 * (i & 3) + 4 * u) * 2, 4 * 272); asm volatile("" : "+a"(W[2 * mat + u][k])); }
  }
  bar();
  WSTAMP;
  CR.sel = get_sel(R, CR.tok, layer, lane);
  int known = 0;
  const int kq = 4 * f + w;
  const unsigned ho0 = C_H + (kq / 14) * 4 * CELL + xmap((kq % 14) * 1024), ho1 = C_H + (kq / 14) * 4 * CELL + xmap((14 + kq % 14) * 1024);
  bool have = false;
  for (int s = 0; s < NSEG; ++s) {
    LAS char* sX = lds + (s & 1) * XSZ;
    STDECL;
    ensure(R, CR.tok, known, layer, s + 1, lane);
    STAMP(s, 0);
    const unsigned cro = crit_off(CR, s), crn = crit_off(CR, s + 1 < NSEG ? s + 1 : s);
    v4u xn[7];
    if (!have) {
      unsigned left = SPIN_CAP;
      for (;;) {
        x_issue(R, cro + C_XN2, tid, xn);
        asm volatile("" ::: "memory");
        SB();
        unsigned m = 0;
#pragma unroll
        for (int k = 0; k < 7; ++k) { sts16(sX + tid * 16 + k * 4096, xn[k]); m = mx4(m, xn[k]); SB(); }
        if (!any_sent(m)) break;
        give_up(left);
        if (F1POLL) poll1(R, (lane & 31) * 128, cro + C_XN2 + xmap(3 * 4096));
      }
    }
    bar();
    STAMP(s, 1);
    const LAS char* xl = opaque(sX + lane * 16);
    LAS char* xd = opaque(lds + ((s & 1) ^ 1) * XSZ + tid * 16);
    f32x4 acc[4][2];
    bf16x8 xf[14];
#pragma unroll
    for (int k = 0; k < 4; ++k) xf[k] = asb8(lds16(xl + k * 1024));
    unsigned o[8], mm = 0; GS ga, gb;
    SB();
    sfor<0, 112>([&](auto N) __attribute__((always_inline)) {
      constexpr int n = decltype(N)::v, ph = n / 28, r = n % 28, ks = r / 2, gu = r & 1;
      if constexpr (ks == 0) m0_av(acc[ph][gu], W[2 * gu + (ph & 1)][ks], xf[ks]); else mm_av(acc[ph][gu], W[2 * gu + (ph & 1)][ks], xf[ks]);
      if constexpr (ph == 0 && gu == 0 && ks + 4 < 14) xf[ks + 4] = asb8(lds16(xl + (ks + 4) * 1024));
      if constexpr (n >= F1X && n < F1X + 21 && (n - F1X) % 3 == 0) xn[(n - F1X) / 3] = x_ld(R, crn + C_XN2, tid, (n - F1X) / 3);
      if constexpr (ph == 1 && gu == 1) xf[ks] = asb8(lds16(xl + (14 + ks) * 1024));
      if constexpr (ph >= 1 && r >= 2 && r < 2 + 2 * GS_STEPS) {
        constexpr int z = r - 2;
        if constexpr (z & 1) glu_step<z / 2>(gb, acc[ph - 1][0], acc[ph - 1][1], 1, o[2 * (ph - 1) + 1]); else glu_step<z / 2>(ga, acc[ph - 1][0], acc[ph - 1][1], 0, o[2 * (ph - 1)]);
      }
      if constexpr (n == 56 + 26) st16(R, lane * 16, cro + ho0, v4u{o[0], o[1], o[2], o[3]});
      SB();
    });
    nop<5>();
    sfor<0, 2 * GS_STEPS>([&](auto N) __attribute__((always_inline)) {
      constexpr int z = decltype(N)::v;
      if constexpr (z % 3 == 2 && z / 3 < 7) { sts16(xd + (z / 3) * 4096, xn[z / 3]); SB(); }
      if constexpr (z & 1) glu_step<z / 2>(gb, acc[3][0], acc[3][1], 1, o[7]); else glu_step<z / 2>(ga, acc[3][0], acc[3][1], 0, o[6]);
    });
    st16(R, lane * 16, cro + ho1, v4u{o[4], o[5], o[6], o[7]});
    STAMP(s, 2); STAMP(s, 11);
    SB();
#pragma unroll
    for (int k = 0; k < 7; ++k) mm = mx4(mm, xn[k]);
    have = !any_sent(mm);
    STFLUSH(s);
  }
}

DI void role_f2(rsrc_t R, const Params& P, LAS char* lds, Crit CR, int layer, int rep, int c2, int tid) {
  const int lane = tid & 63, w = __builtin_amdgcn_readfirstlane(tid >> 6), i = lane & 15, g = lane >> 4;
  bf16x8 W[14][4];
#pragma unroll
  for (int hf = 0; hf < 2; ++hf) {
    const u16* src = P.wd + (size_t)(448 * w + 224 * hf + (lane >> 3)) * DM + 64 * c2 + 8 * (lane & 7);
    LAS char* buf = lds + w * (224 * 144);
    v4u t[28];
#pragma unroll
    for (int k = 0; k < 28; ++k) t[k] = *(const v4u*)(src + (size_t)k * 8 * DM);
    if (hf) bar();
#pragma unroll
    for (int k = 0; k < 28; ++k) sts16(buf + ((lane >> 3) + 8 * k) * 144 + (lane & 7) * 16, t[k]);
    bar();
#pragma unroll
    for (int k = 0; k < 7; ++k)
#pragma unroll
      for (int u = 0; u < 4; ++u) { W[7 * hf + k][u] = tr_frag(buf + (32 * k + 8 * g + (i >> 2)) * 144 + (16 * u + 4 * (i & 3)) * 2, 4 * 144); asm volatile("" : "+a"(W[7 * hf + k][u])); }
  }
  bar();
  WSTAMP;
  CR.sel = get_sel(R, CR.tok, layer, lane);
  int known = 0;
  const int row = 16 * rep + i, col = 16 * w + 4 * g;
  const unsigned xo = xmap((4 * c2 + (col >> 4)) * 1024) + row * 32 + (col & 15) * 2;
  const unsigned pv = xmap((14 * rep + (lane < 14 ? lane : 13)) * 1024) + 1008;
  unsigned ho[4];
#pragma unroll
  for (int q = 0; q < 4; ++q) ho[q] = C_H + w * 4 * CELL + xmap(((14 * rep + 4 * q) & ~3) * 1024);
  for (int s = 0; s < NSEG; ++s) {
    const unsigned cro = crit_off(CR, s);
    STDECL;
    ensure(R, CR.tok, known, layer, s, lane);
    STAMP(s, 0);
    f32x4 acc[4];
    poll1(R, pv, cro + C_H + w * 4 * CELL);
    STAMP(s, 1);
    v2u res;
    unsigned left = SPIN_CAP;
    for (;;) {
      v4u hh[14];
      if (rep == 0) {
#pragma unroll
        for (int k = 0; k < 14; ++k) { hh[k] = ld16(R, lane * 16 + (k & 3) * 1024, cro + ho[k >> 2]); SB(); }
      } else {
#pragma unroll
        for (int k = 0; k < 14; ++k) { hh[k] = ld16(R, lane * 16 + ((k + 2) & 3) * 1024, cro + ho[(k + 2) >> 2]); SB(); }
      }
      res = ld8(R, xo, cro + C_X1);
      asm volatile("" ::: "memory");
      SB();
      unsigned mm = 0;
#pragma unroll
      for (int k = 0; k < 14; ++k) {
#pragma unroll
        for (int u = 0; u < 4; ++u) { if (k == 0) m0_av(acc[u], W[k][u], asb8(hh[k])); else mm_av(acc[u], W[k][u], asb8(hh[k])); if (u == 1) a_mx4(mm, hh[k]); }
      }
      if (!any_sent(mm)) break;
      give_up(left);
      nop<7>();
    }
    nop<3>();
    LAS char* sO = lds + (s & 1) * 16384;
#pragma unroll
    for (int u = 0; u < 4; ++u) { pin(acc[u]); *(LAS f32x4*)(sO + (u * 4 + w) * 1024 + lane * 16) = acc[u]; }
    STAMP(s, 2);
    bar();
    const LAS char* ra = sO + w * 4096 + lane * 16;
    const f32x4 a = (*(const LAS f32x4*)(ra) + *(const LAS f32x4*)(ra + 1024)) + (*(const LAS f32x4*)(ra + 2048) + *(const LAS f32x4*)(ra + 3072));
    const unsigned y0 = pk2(a[0], a[1]), y1 = pk2(a[2], a[3]);
    v2u o = {pk2(b2f_lo(res.x) + b2f_lo(y0), b2f_hi(res.x) + b2f_hi(y0)), pk2(b2f_lo(res.y) + b2f_lo(y1), b2f_hi(res.y) + b2f_hi(y1))};
    if (layer == 0) st8(R, xo, cro + C_XIO, v2u{clean(o.x), clean(o.y)});
    else *(v2u*)(P.out + (size_t)(s * 32 + row) * DM + 64 * c2 + col) = o;
    STAMP(s, 3); STAMP(s, 11); STFLUSH(s);
  }
  if (layer == 1 && rep == 1 && c2 == 0) st4(R, tid == 0 ? 0u : 0x80000000u, K_E, CR.tok);
}

DI void fill_cell(rsrc_t R, unsigned half, int s, int ci, int tid) {
  const v4u sv = {SENT, SENT, SENT, SENT};
  const int cell = ci < 20 ? (ci ? ci + 1 : 0) : (ci < 23 ? ci + 5 : ci + 9);
  if (s == 0 && cell == 0) return;
  st16(R, tid * 16, half + cell * CELL, sv);
  if (!(cell == 12 || cell == 16 || cell == 20 || (cell >= 32 && (cell & 3) == 3))) st16(R, tid * 16 + 4096, half + cell * CELL, sv);
}
DI void role_fill(rsrc_t R, Crit CR, int layer, int p, int tid) {
  CR.sel = get_sel(R, CR.tok, layer, tid & 63);
  wait_all(R, CR.tok, tid & 63);
  for (int t = 0; t < 32; ++t) {
    const int l = t >= 3 && ((t & 1) || t > 28), r = l ? (t < 29 ? (t - 3) >> 1 : t - 16) : (t < 3 ? t : (t + 2) >> 1);
    if (r) for (int u = p; u < 4 * 39; u += NFILL) fill_cell(R, crit_off(CR, 4 * r + u / 39, (unsigned)(l ^ layer)), 1, u % 39, tid);
    asm volatile("s_waitcnt vmcnt(0)\n\ts_barrier" ::: "memory");
    st4(R, tid == 0 ? p * 4 : 0x80000000u, K_FILL, CR.tok + t + 1);
  }
}

struct ABL { rsrc_t rn, rk; unsigned vr, vo, sn, sk, second; int q4; };
constexpr unsigned WROW = NH * DH * 2;
template <int isB> DI ABL ab_setup(const Params& P, int h, int kh, int q4, int tid, int w) {
  const int lane = tid & 63, r8 = lane >> 3, pc = lane & 7;
  ABL l;
  l.rn = __builtin_amdgcn_make_buffer_rsrc((void*)(isB ? P.Wo : P.Wq), 0, DM * WROW, 0x00020000);
  l.rk = __builtin_amdgcn_make_buffer_rsrc((void*)(isB ? P.Wv : P.Wk), 0, DM * WROW, 0x00020000);
  l.vr = (8 * (r8 >> 2) + (r8 & 3)) * WROW + 16 * pc;
  l.vo = (tid & 15) < 14 ? (tid >> 4) * (DM * 2) + (tid & 15) * 16 : 0x80000000u;
  l.sn = isB ? h * DH * (DM * 2) + 224 * q4 : h * 512 + 128 * w;
  l.sk = 32 * (7 * kh + 2 * w) * WROW + h * 512; l.second = w < 3 ? 32 * WROW : 0x40000000u; l.q4 = q4;
  return l;
}
template <int isB, int k> DI void ab_ld(const ABL& l, v4u (&a)[32], v4u (&st)[16]) {
  if constexpr (k < 32) {
    a[k] = __builtin_amdgcn_raw_buffer_load_b128(l.rk, l.vr + 128 * (k & 3), l.sk + (k >> 4) * l.second + (4 * ((k >> 3) & 1) + 16 * ((k >> 2) & 1)) * WROW, 2);
  } else if constexpr (isB) st[k - 32] = __builtin_amdgcn_raw_buffer_load_b128(l.rn, l.vo, l.sn + 16 * (k - 32) * (DM * 2), 2);
  else if constexpr (k < 46) {
    constexpr int u = (k - 32) >> 1;
    const int n0 = (u < 6 ? 32 * (3 * l.q4 + (u >> 1)) + 4 * (u & 1) : 32 * (12 + (l.q4 >> 1)) + 4 * (l.q4 & 1)) + 16 * (k & 1);
    st[k - 32] = __builtin_amdgcn_raw_buffer_load_b128(l.rn, l.vr, l.sn + n0 * WROW, 2);
  }
  SB();
}
template <int isB> DI void ab_stage(LAS char* lds, int tid, int w, const v4u (&a)[32], const v4u (&st)[16], bf16x8 (&f)[32]) {
  constexpr int FR = 1088, TILE = 8 * FR;
  const int lane = tid & 63, r8 = lane >> 3, pc = lane & 7, i = lane & 15, g = lane >> 4;
  LAS char* own = lds + 7 * TILE + 1024 + w * 2 * TILE;
  LAS char* sh = opaque(lds + (isB ? (tid >> 4) * 240 + ((tid & 15) < 14 ? (tid & 15) : 14) * 16 : (8 * w + pc) * 272 + r8 * 16));
  LAS char* ow = opaque(own + pc * 272 + r8 * 16);
  const LAS char* orr = opaque(own + g * 272 + i * 16);
#pragma unroll
  for (int v = 0; v < 4; ++v) {
    if (v < 2 || w < 3)
#pragma unroll
    for (int k = 0; k < 8; ++k) sts16(ow + (v & 1) * TILE + 8 * (k & 3) * 272 + (k >> 2) * 128, a[8 * v + k]);
    asm volatile("" ::: "memory");
#pragma unroll
    for (int es = 0; es < 8; ++es) f[8 * v + es] = asb8(lds16(orr + (v & 1) * TILE + es * FR));
    asm volatile("" ::: "memory");
  }
#pragma unroll
  for (int k = 0; k < (isB ? 16 : 14); ++k) sts16(sh + (isB ? k * 16 * 240 : (k >> 1) * TILE + (k & 1) * 128), st[k]);
}
template <int isB> DI void ab_mul(rsrc_t R, LAS char* lds, int h, int kh, int q4, int tid, int w, const bf16x8 (&f)[32]) {
  constexpr int FR = 1088, TILE = 8 * FR;
  const int lane = tid & 63, i = lane & 15, g = lane >> 4;
  const LAS char* rb = opaque(lds + (isB ? (8 * g + (i >> 2)) * 240 + 8 * (i & 3) : g * 272 + i * 16));
  const unsigned so2 = w < 3 ? lane * 16 + 1024 : 0x80000000u;
  const unsigned off0 = (isB ? WB_OFF + (unsigned)((7 * q4 * 4 + (h >> 2)) * 4 + (h & 3)) * 14336 : WA_OFF + (unsigned)((h * 4 + q4) * 7) * 14336) + (7 * kh + 2 * w) * 1024;
  constexpr float sc = 0.0625f * LOG2E;
  f32x4 c[2][4];
  float m0, m1; v4u o;
#define AB_RD(n) (isB ? tr_frag(rb + 32 * ((n) & 7) * 240 + 32 * ((n) >> 3), 4 * 240) : asb8(lds16(rb + ((n) >> 3) * TILE + ((n) & 7) * FR)))
  bf16x8 nx[3] = {AB_RD(0), AB_RD(1), AB_RD(2)};
  sfor<0, 256>([&](auto N) __attribute__((always_inline)) {
    constexpr int m = decltype(N)::v, n = m >> 2, q = m & 3, u = n >> 3, es = n & 7;
    if constexpr (n < 56) {
      if constexpr (es == 0) m0_av(c[u & 1][q], f[8 * q + es], nx[n % 3]); else mm_av(c[u & 1][q], f[8 * q + es], nx[n % 3]);
      if constexpr (q == 3 && n + 3 < 56) { nx[n % 3] = AB_RD(n + 3); SB(); }
    } else if constexpr (m == 224) nop<7>();
    if constexpr (u >= 1 && (q == 1 || q == 2)) {
      const f32x4& cc = c[(u - 1) & 1][es >> 1];
      if constexpr (q == 1) { if constexpr (!isB) { m0 = a_mulc(cc[2 * (es & 1)], sc); m1 = a_mulc(cc[2 * (es & 1) + 1], sc); } }
      else {
        if constexpr (isB) o[es & 3] = cvt(cc[2 * (es & 1)], cc[2 * (es & 1) + 1]); else o[es & 3] = cvt(m0, m1);
        if constexpr ((es & 3) == 3) st16(R, es >> 2 ? so2 : lane * 16, off0 + (u - 1) * (isB ? 16 * 14336 : 14336), o);
      }
    }
    SB();
  });
}
#define ABST(k)
template <int isB> DI void start_ab(rsrc_t R, const Params& P, const Crit& CR, LAS char* lds, unsigned ew, int q, int tid) {
  const int w = __builtin_amdgcn_readfirstlane(tid >> 6), h = q >> 3, kh = q & 1, q4 = (q >> 1) & 3, id = 128 * isB + q;
  const int lane = tid & 63, row = id * 8 + (lane & 7), sub = lane >> 3;
  v4u a[32], st[16], x[7];
  bf16x8 f[32];
  ABST(0);
  const rsrc_t rx = __builtin_amdgcn_make_buffer_rsrc((void*)P.x, 0, SQ * DM * 2, 0x00020000);
#pragma unroll
  for (int k = 0; k < 7; ++k) { x[k] = ld16p(rx, w == 3 ? row * (DM * 2) + 16 * sub + 128 * k : 0x80000000u, 0); SB(); }
  const ABL la = ab_setup<isB>(P, h, kh, q4, tid, w);
  sfor<0, 48>([&](auto N) __attribute__((always_inline)) { ab_ld<isB, decltype(N)::v>(la, a, st); });
  ABST(1);
  if (w == 3) {
    float sum = 0;
#pragma unroll
    for (int k = 0; k < 7; ++k)
#pragma unroll
      for (int e = 0; e < 4; ++e) sum += b2f_lo(x[k][e]) + b2f_hi(x[k][e]);
    sum += dpp<XOR8>(sum);
    const float mean = x32_add(x16_add(sum)) * (1.0f / DM);
    float sq = 0;
#pragma unroll
    for (int k = 0; k < 7; ++k)
#pragma unroll
      for (int e = 0; e < 4; ++e) { const float p = b2f_lo(x[k][e]) - mean, z = b2f_hi(x[k][e]) - mean; sq += p * p + z * z; }
    sq += dpp<XOR8>(sq);
    const float var = x32_add(x16_add(sq)) * (1.0f / DM);
    const float mb = rbf(mean), rs = ln_rs(var);
#pragma unroll
    for (int k = 0; k < 7; ++k)
#pragma unroll
      for (int e = 0; e < 4; ++e) x[k][e] = clean(ln_word(x[k][e], mb, rs));
  }
  ab_stage<isB>(lds, tid, w, a, st, f);
  if (w == 3) {
#pragma unroll
    for (int k = 0; k < 7; ++k) {
      const unsigned vo = xmap(((row >> 4) & 1) * 14336 + (sub + 8 * k) * 256 + (row & 15) * 16);
      st16(R, vo, CR.base + (id >> 2) * GRP + C_XN0, x[k]); st16(R, vo, CR.base + (id >> 2) * GRP + C_XN0 + 8192, x[k]);
    }
  }
  for (int u = 255 - id; u < 8 * 39; u += 256) fill_cell(R, CR.base + (u / 78) * GRP + (u / 39 & 1) * 8192, u / 78, u % 39, tid);
  if (id == 255) {
    for (int t = tid; t < 448; t += 256) {
      const unsigned blk = t >> 4, qq = t & 15;
      const unsigned* src = (const unsigned*)(P.mem0 + ((qq >> 2) * 2 + (qq & 1)) * DM + 16 * blk) + ((qq >> 1) & 1);
      const v4u v = {clean(src[0]), clean(src[2]), clean(src[4]), clean(src[6])};
      st16(R, t * 16, CR.base + C_MEMA, v); st16(R, t * 16, CR.base + C_MEMA + 8192, v);
    }
  }
  bar();
  ABST(2);
  ab_mul<isB>(R, lds, h, kh, q4, tid, w, f);
  ABST(3);
  post_tok(R, K_AB + id * 4, ew + 64u, tid);
  ABST(4);
}
DI void start_probe(rsrc_t R, const Crit& CR, int p, int tid) {
  if (tid < 64) {
    const unsigned base = CR.base + 64 * GRP, va = CR.va + 64 * GRP;
    unsigned best[2] = {0x7fffffffu, 0x7fffffffu};
    for (int r = 0; r < 5; ++r)
#pragma unroll
      for (int hf = 0; hf < 2; ++hf) {
        const unsigned t0 = now();
        st4(R, tid < 32 ? tid * 4 : 0x80000000u, base + hf * 8192 + p * 256, 0u);
        asm volatile("s_waitcnt vmcnt(0)" ::: "memory");
        const unsigned d = now() - t0;
        best[hf] = d < best[hf] ? d : best[hf];
      }
    const unsigned die = (((unsigned)__builtin_amdgcn_s_getreg(20 | (3 << 11)) & 0xfu) >> 2) & 1u;
    int v = (int)best[0] - (int)best[1];
    v = v > 300 ? 300 : (v < -300 ? -300 : v);
    if ((die ^ (va >> 21)) & 1u) v = -v;
    st8(R, tid == 0 ? p * 8 : 0x80000000u, K_CAL, v2u{(unsigned)v, CR.tok});
  }
}

constexpr int MAIN_LDS = 57344 + 2 * XSZ + 8192 + 20480 + 7168;

__global__ void __launch_bounds__(256, 1) k_main(Params P) {
  extern __shared__ __attribute__((aligned(16))) char smem[];
  LAS char* lds = (LAS char*)smem;
  const int tid = threadIdx.x, b = blockIdx.x;
  const int slot = (b & 7) * 32 + (b >> 3);
  const int layer = slot >> 7;
  int xl = (slot >> 5) & 3, c = slot & 31;
  if (PSPLIT && (xl == 0 || xl == 3)) {
    const int second = xl == 3;
    if (c < 16) { xl = 0; c = PSPLIT == 1 ? c + 16 * second : (c >> 3) * 16 + 8 * second + (c & 7); }
    else { xl = 3; c = c >= 30 ? 28 + 2 * second + (c - 30) : (FMIX ? (c < 23 ? c - 16 : c - 9) + 7 * second : c - 16 + 14 * (1 - second)); }
  }
  const rsrc_t R = __builtin_amdgcn_make_buffer_rsrc(P.ws, 0, WS_END, 0x00020000);
  Crit CR;
  CR.base = crit_base(P.ws); CR.va = (unsigned)(size_t)P.ws + CR.base;
  const unsigned t0 = now();
  const unsigned ew = __builtin_amdgcn_raw_buffer_load_b32(R, 0, K_E, 16);
  CR.sel = 0;
  {
    const bool fw = xl == 3 && c < 28;
    const int px = slot >> 5, pc = slot & 31;
    const bool mixed = px == 0 || px == 3;
    if (mixed ? pc < 16 : (px >= 4 && px < 7)) start_ab<0>(R, P, CR, lds, ew, mixed ? (px ? 16 : 0) + pc : (px - 3) * 32 + pc, tid);
    else {
      const unsigned wc = warm_code(c, tid);
      while ((int)(now() - t0) < BWAIT) __builtin_amdgcn_s_sleep(4);
      start_ab<1>(R, P, CR, lds, ew, mixed ? (px ? 0 : -16) + pc : (px == 7 ? 3 : px) * 32 + pc, tid);
      asm volatile("" :: "v"(wc));
    }
    CR.tok = __builtin_amdgcn_readfirstlane(ew) + 64u;
    if (layer == 1 && (xl == 1 || xl == 2) && c >= 28) start_probe(R, CR, (xl - 1) * 4 + c - 28, tid);
    if (xl == 0 || c < 28) { const int hold = fw ? (layer ? F1W : FWAIT) : (layer ? L1WAIT : 0); while ((int)(now() - t0) < hold) __builtin_amdgcn_s_sleep(8); }
  }
  if (xl == 0) role_p(R, lds, CR, layer, c >> 4, c & 15, tid);
  else if (xl < 3) {
    if (c < 28) role_m(R, P, lds, CR, layer, MSPLIT ? c / 14 : xl - 1, MSPLIT ? (xl - 1) * 14 + c % 14 : c, tid);
    else if (xl == 1 || layer == 1) role_n(R, lds, CR, layer, xl == 1 ? C_X1 : C_XIO, xl == 1 ? 0u : 1u, xl == 1 ? C_XN2 : C_XIO, c - 28, tid);
    else role_fill(R, CR, layer, c - 28, tid);
  } else {
    if (c < 14) role_f1(R, P, lds, CR, layer, c, tid);
    else if (c < 28) role_f2(R, P, lds, CR, layer, (c - 14) / 7, (c - 14) % 7, tid);
    else role_fill(R, CR, layer, 4 + 4 * layer + c - 28, tid);
  }
}

__global__ void k_end() {}

extern "C" void kernel_launch(void* const* d_in, const int* in_sizes, int n_in, void* d_out, int out_size, void* d_ws, size_t ws_size, hipStream_t stream) {
  Params P;
  P.x = (const u16*)d_in[0]; P.mem0 = (const u16*)d_in[1];
  P.Wq = (const u16*)d_in[2]; P.Wk = (const u16*)d_in[3]; P.Wv = (const u16*)d_in[4]; P.Wo = (const u16*)d_in[5];
  P.wg = (const u16*)d_in[8]; P.wu = (const u16*)d_in[9]; P.wd = (const u16*)d_in[10];
  P.out = (u16*)d_out; P.ws = (char*)d_ws + (WS_TOP ? ((ws_size - WS_END) & ~(size_t)4095) : 0);
  (void)hipFuncSetAttribute((const void*)k_main, hipFuncAttributeMaxDynamicSharedMemorySize, MAIN_LDS);
  k_main<<<256, 256, MAIN_LDS, stream>>>(P);
}
```
